```python
import torch

def _softmax(x, axis=-1):
    return torch.softmax(x, dim=axis)
def _gelu(x):
    return torch.nn.functional.gelu(x, approximate="tanh")
def _silu(x):
    return torch.nn.functional.silu(x)
def _sigmoid(x):
    return torch.sigmoid(x)
def _layernorm(x, axis=-1, eps=1e-5):
    m = x.mean(dim=axis, keepdim=True)
    v = x.var(dim=axis, keepdim=True, unbiased=False)
    return (x - m) / torch.sqrt(v + eps)
def _scatter_add(tgt, idx, src, axis=0):
    return tgt.clone().index_add_(axis, idx, src)
def _segment_sum(src, seg, n):
    out = torch.zeros((n,) + src.shape[1:], dtype=src.dtype)
    return out.index_add_(0, seg, src)
def _pad(x, pw):
    flat = tuple(v for pair in reversed(pw) for v in pair)
    return torch.nn.functional.pad(x, flat)

kernel_name = 'parallel_sum__linear_kernel_hedgehog__dense_activation_mish__prompt'

def setup_inputs():

    _w = (lambda a: (lambda t: t.to(torch.bfloat16) if t.is_floating_point() else t)(torch.as_tensor(a)))
    def _A_setup():
        B, S, D, H = 1, 2048, 448, 16
        Dh = D // H
        return {
            "x": _w((lambda *s: torch.randn(*s, dtype=torch.bfloat16) * 0.05)(B, S, D)),
            "wqkv": _w((lambda *s: torch.randn(*s, dtype=torch.bfloat16) * 0.2)(D, 3 * D)),
            "w_phi": _w((lambda *s: torch.randn(*s, dtype=torch.bfloat16) * 0.2)(H, Dh, Dh) * (Dh ** -0.5)),
            "wo": _w((lambda *s: torch.randn(*s, dtype=torch.bfloat16) * 0.05)(D, D)),
        }
    _A_in = _A_setup()

    def _B_setup():
        B, S, D, F = 1, 2048, 448, 1792
        return {
            "x": _w((lambda *s: torch.randn(*s, dtype=torch.bfloat16) * 0.05)(B, S, D)),
            "w1": _w((lambda *s: torch.randn(*s, dtype=torch.bfloat16) * 0.8)(D, F) * (D ** -0.5)),
            "b1": _w((lambda *s: torch.randn(*s, dtype=torch.bfloat16) * 0.4)(F)),
            "w2": _w((lambda *s: torch.randn(*s, dtype=torch.bfloat16) * 0.1)(F, D) * (F ** -0.5)),
        }
    _B_in = _B_setup()

    return {
        "A_x": _A_in["x"],
        "A_wqkv": _A_in["wqkv"],
        "A_w_phi": _A_in["w_phi"],
        "A_wo": _A_in["wo"],
        "B_x": _B_in["x"],
        "B_w1": _B_in["w1"],
        "B_b1": _B_in["b1"],
        "B_w2": _B_in["w2"],
    }

def reference(A_x, A_wqkv, A_w_phi, A_wo, B_x, B_w1, B_b1, B_w2):
    def _A_ref(x, wqkv, w_phi, wo):
        B, S, D = x.shape
        H = 16; Dh = D // H
        qkv = (lambda x, *d: x.permute(*d))((x @ wqkv).reshape(B, S, 3, H, Dh), 2, 0, 3, 1, 4)
        q, k, v = qkv[0], qkv[1], qkv[2]
        pq = torch.einsum("bhsd,hdf->bhsf", q, w_phi)
        pk = torch.einsum("bhsd,hdf->bhsf", k, w_phi)
        phi_q = torch.concatenate([_softmax(pq, axis=-1), _softmax(-pq, axis=-1)], axis=-1)
        phi_k = torch.concatenate([_softmax(pk, axis=-1), _softmax(-pk, axis=-1)], axis=-1)
        kv = torch.einsum("bhsf,bhsd->bhfd", phi_k, v)
        z  = torch.einsum("bhsf->bhf", phi_k)
        num = torch.einsum("bhqf,bhfd->bhqd", phi_q, kv)
        den = torch.einsum("bhqf,bhf->bhq", phi_q, z)[..., None] + 1e-6
        o = num / den
        return (lambda x, *d: x.permute(*d))(o, 0, 2, 1, 3).reshape(B, S, D) @ wo

    def _B_ref(x, w1, b1, w2):
        h = torch.einsum("bsd,df->bsf", x, w1) + b1
        sp = torch.log1p(torch.exp(-torch.abs(h))) + torch.where(h > 0, h, torch.zeros_like(h))
        act = h * torch.tanh(sp)
        return torch.einsum("bsf,fd->bsd", act, w2)

    x = A_x
    xn = _layernorm(x, axis=-1)
    return x + _A_ref(xn, A_wqkv, A_w_phi, A_wo) + _B_ref(xn, B_w1, B_b1, B_w2)
```

```cpp
#include <hip/hip_runtime.h>
#include <hip/hip_bf16.h>
#include <stdint.h>

typedef unsigned short u16;
constexpr int S = 2048, D = 448, H = 16, DH = 28, F = 1792;

__device__ __forceinline__ float bf2f(u16 v) { return __uint_as_float(((unsigned)v) << 16); }
__device__ __forceinline__ u16 f2bf(float f) { __hip_bfloat16 b = __float2bfloat16(f); return *(u16*)&b; }

__global__ void k_ln(const u16* x, u16* xn) {
    int t = blockIdx.x; int lane = threadIdx.x;
    float v[7]; float s = 0;
    for (int i = 0; i < 7; ++i) { v[i] = bf2f(x[t * D + lane + 64 * i]); s += v[i]; }
    for (int m = 32; m >= 1; m >>= 1) s += __shfl_xor(s, m);
    float mean = s / D; float q = 0;
    for (int i = 0; i < 7; ++i) { float d = v[i] - mean; q += d * d; }
    for (int m = 32; m >= 1; m >>= 1) q += __shfl_xor(q, m);
    float r = rsqrtf(q / D + 1e-5f);
    for (int i = 0; i < 7; ++i) xn[t * D + lane + 64 * i] = f2bf((v[i] - mean) * r);
}
template <int MODE>
__global__ void k_gemm(const u16* A, const u16* B, const u16* bias, u16* C, int N, int K) {
    int n = blockIdx.x * 64 + threadIdx.x; int t = blockIdx.y;
    if (n >= N) return;
    float acc = 0;
    for (int k = 0; k < K; ++k) acc += bf2f(A[t * K + k]) * bf2f(B[k * N + n]);
    if (MODE == 1) {
        float h = acc + bf2f(bias[n]);
        float e = __expf(fminf(h, 20.f)); float tt = e * (e + 2.f);
        acc = h * tt / (tt + 2.f);
    }
    C[t * N + n] = f2bf(acc);
}
__global__ void k_phi(const u16* qkv, const u16* wphi, u16* phi) {
    int t = blockIdx.x; int i = threadIdx.x;
    if (i >= 32) return;
    int which = i >> 4, h = i & 15;
    float p[DH]; float mx = -1e30f, mn = 1e30f;
    for (int f = 0; f < DH; ++f) {
        float a = 0;
        for (int d = 0; d < DH; ++d) a += bf2f(qkv[t * 3 * D + which * D + h * DH + d]) * bf2f(wphi[h * DH * DH + d * DH + f]);
        p[f] = a; mx = fmaxf(mx, a); mn = fminf(mn, a);
    }
    float s1 = 0, s2 = 0;
    for (int f = 0; f < DH; ++f) { s1 += __expf(p[f] - mx); s2 += __expf(mn - p[f]); }
    for (int f = 0; f < DH; ++f) {
        phi[((t * 2 + which) * H + h) * 56 + f] = f2bf(__expf(p[f] - mx) / s1);
        phi[((t * 2 + which) * H + h) * 56 + 28 + f] = f2bf(__expf(mn - p[f]) / s2);
    }
}
__global__ void k_kv(const u16* qkv, const u16* phi, float* kv) {
    int h = blockIdx.x, f = blockIdx.y, d = threadIdx.x;
    if (d > 28) return;
    float a = 0;
    for (int t = 0; t < S; ++t) {
        float v = d < 28 ? bf2f(qkv[t * 3 * D + 2 * D + h * DH + d]) : 1.f;
        a += bf2f(phi[((t * 2 + 1) * H + h) * 56 + f]) * v;
    }
    kv[(h * 56 + f) * 29 + d] = bf2f(f2bf(a));
}
__global__ void k_o(const u16* phi, const float* kv, u16* o) {
    int t = blockIdx.x; int i = threadIdx.x;
    int h = i / DH, d = i % DH;
    float num = 0, den = 0;
    for (int f = 0; f < 56; ++f) {
        float p = bf2f(phi[((t * 2 + 0) * H + h) * 56 + f]);
        num += p * kv[(h * 56 + f) * 29 + d]; den += p * kv[(h * 56 + f) * 29 + 28];
    }
    o[t * D + i] = f2bf(num / (den + 1e-6f));
}
__global__ void k_out(const u16* x, const u16* o, const u16* wo, const u16* act, const u16* w2, u16* out) {
    int n = blockIdx.x * 64 + threadIdx.x; int t = blockIdx.y;
    float a = 0, b = 0;
    for (int k = 0; k < D; ++k) a += bf2f(o[t * D + k]) * bf2f(wo[k * D + n]);
    for (int k = 0; k < F; ++k) b += bf2f(act[t * F + k]) * bf2f(w2[k * D + n]);
    out[t * D + n] = f2bf(bf2f(x[t * D + n]) + a + b);
}

extern "C" void kernel_launch(void* const* d_in, const int* in_sizes, int n_in,
                              void* d_out, int out_size, void* d_ws, size_t ws_size,
                              hipStream_t stream) {
    const u16* x = (const u16*)d_in[0];
    const u16* wqkv = (const u16*)d_in[1];
    const u16* wphi = (const u16*)d_in[2];
    const u16* wo = (const u16*)d_in[3];
    const u16* w1 = (const u16*)d_in[5];
    const u16* b1 = (const u16*)d_in[6];
    const u16* w2 = (const u16*)d_in[7];
    char* ws = (char*)d_ws;
    u16* xn = (u16*)ws; ws += S * D * 2;
    u16* qkv = (u16*)ws; ws += S * 3 * D * 2;
    u16* act = (u16*)ws; ws += S * F * 2;
    u16* phi = (u16*)ws; ws += S * 2 * H * 56 * 2;
    float* kv = (float*)ws; ws += H * 56 * 29 * 4 + 64;
    u16* o = (u16*)ws; ws += S * D * 2;
    k_ln<<<S, 64, 0, stream>>>(x, xn);
    k_gemm<0><<<dim3(3 * D / 64, S), 64, 0, stream>>>(xn, wqkv, nullptr, qkv, 3 * D, D);
    k_gemm<1><<<dim3(F / 64, S), 64, 0, stream>>>(xn, w1, b1, act, F, D);
    k_phi<<<S, 64, 0, stream>>>(qkv, wphi, phi);
    k_kv<<<dim3(H, 56), 64, 0, stream>>>(qkv, phi, kv);
    k_o<<<S, 448, 0, stream>>>(phi, kv, o);
    k_out<<<dim3(D / 64, S), 64, 0, stream>>>(x, o, wo, act, w2, (u16*)d_out);
}
```

```cpp
#pragma clang diagnostic ignored "-Wint-to-pointer-cast"
#include <hip/hip_runtime.h>
#include <stdint.h>

typedef unsigned short u16;
typedef unsigned int u32;
typedef unsigned long long u64;
typedef __attribute__((ext_vector_type(8))) __bf16 bf16x8;
typedef __attribute__((ext_vector_type(4))) short s16x4;
typedef __attribute__((ext_vector_type(8))) short s16x8;
typedef __attribute__((ext_vector_type(16))) float f32x16;
typedef __attribute__((ext_vector_type(4))) float f32x4;
typedef __attribute__((ext_vector_type(2))) float f32x2;
typedef __attribute__((ext_vector_type(4))) u32 u32x4;
typedef __attribute__((ext_vector_type(2))) u32 u32x2;
#define LAS __attribute__((address_space(3)))
#define GAS __attribute__((address_space(1)))
#define DI __device__ __forceinline__

constexpr u32 WS_OFLAG = 4096, WS_AFLAG = WS_OFLAG + 16384, WS_KVFLAG = 65536, WS_KVP = 131072, WS_ACT = 2u << 20;
constexpr u32 WS_DBG = 16u << 20;
constexpr u32 L_A = 0, L_B = 57344, L_X = 57344, L_PHI = 114688, L_V = 57344, L_WPHI = 155648, L_STAT = 159744, L_CS = 161792;
constexpr u32 LDS_BYTES = 163840;
constexpr float LOG2E = 1.4426950408889634f;

struct Params { const char *x, *wqkv, *wphi, *wo, *w1, *b1, *w2; char* out; char* ws; };

#define STAMP(i)

DI void dma16(u32 voff, const char* gbase, u32 ldsw, u32 imm) {
    asm volatile("s_add_u32 m0, %1, %2\n\ts_nop 0\n\tglobal_load_lds_dwordx4 %0, %3"
                 :: "v"(voff), "s"(ldsw), "i"(imm), "s"(gbase) : "memory", "scc");
}
#define WAITV(n) asm volatile("s_waitcnt vmcnt(" #n ")" ::: "memory")
DI void bar() { asm volatile("s_waitcnt lgkmcnt(0)\n\ts_barrier" ::: "memory"); }
DI void st16(u32 voff, char* gbase, u32x4 v) {
    asm volatile("global_store_dwordx4 %0, %1, %2 sc1\n\ts_nop 1" :: "v"(voff), "v"(v), "s"(gbase) : "memory");
}
DI void stflag(char* gaddr, u32 tag) {
    u32 z = 0;
    asm volatile("global_store_dword %0, %1, %2 sc1" :: "v"(z), "v"(tag), "s"(gaddr) : "memory");
}
DI void poll(u32 voff, const char* gbase, u32 tag) {
#pragma unroll 1
    for (u32 n = 0; n < (1u << 18); ++n) {
        u32 v;
        asm volatile("global_load_dword %0, %1, %2 sc1\n\ts_waitcnt vmcnt(0)" : "=v"(v) : "v"(voff), "s"(gbase) : "memory");
        if (__builtin_amdgcn_ballot_w64(v != tag) == 0) break;
        __builtin_amdgcn_s_sleep(1);
    }
}
typedef __attribute__((ext_vector_type(2))) __bf16 bf16x2;
DI u32 cvtpk(float lo, float hi) { f32x2 v = {lo, hi}; return __builtin_bit_cast(u32, __builtin_convertvector(v, bf16x2)); }
DI s16x4 trd(u32 a) { return __builtin_amdgcn_ds_read_tr16_b64_v4i16((LAS s16x4*)a); }
DI bf16x8 trd2(u32 a0, u32 a1) {
    s16x4 lo = trd(a0), hi = trd(a1);
    return __builtin_bit_cast(bf16x8, __builtin_shufflevector(lo, hi, 0, 1, 2, 3, 4, 5, 6, 7));
}
DI f32x16 mfma32(bf16x8 a, bf16x8 b, f32x16 c) { return __builtin_amdgcn_mfma_f32_32x32x16_bf16(a, b, c, 0, 0, 0); }
DI f32x4 mfma16(bf16x8 a, bf16x8 b, f32x4 c) { return __builtin_amdgcn_mfma_f32_16x16x32_bf16(a, b, c, 0, 0, 0); }
DI bf16x8 asbf(u32x4 v) { return __builtin_bit_cast(bf16x8, v); }
DI float lo16(u32 v) { return __uint_as_float(v << 16); }
DI float hi16(u32 v) { return __uint_as_float(v & 0xffff0000u); }
DI void halves(float v, float& a, float& b) {
    auto r = __builtin_amdgcn_permlane32_swap(__float_as_uint(v), __float_as_uint(v), false, false);
    a = __uint_as_float(r[0]); b = __uint_as_float(r[1]);
}
template <int S> DI u32x4 pack8(const f32x16& v) {
    u32x4 r;
    r[0] = cvtpk(v[8 * S + 0], v[8 * S + 1]); r[1] = cvtpk(v[8 * S + 2], v[8 * S + 3]);
    r[2] = cvtpk(v[8 * S + 4], v[8 * S + 5]); r[3] = cvtpk(v[8 * S + 6], v[8 * S + 7]);
    return r;
}

struct Ctx {
    int lane, w, tg, hf, h, t32;
    u32 w1k, w2k;
    u32 wa[4];
    u32 vw;
    bf16x8 xf[28];
    bf16x8 mask;
    bf16x8 corrB;
    float rstd;
};

template <int C, u32 BASE> DI void chunk_mma(Ctx& c, f32x16& acc, f32x4& cs) {
#pragma unroll
    for (int j = 0; j < 4; ++j) {
        u32 a = c.wa[j] + BASE;
        bf16x8 wf = trd2(a + C * 8192, a + C * 8192 + 512);
        acc = mfma32(wf, c.xf[4 * C + j], acc);
        if (j == 0) cs = mfma16(c.mask, wf, cs);
    }
}
template <int PAR> DI void cs_write(const Ctx& c, const f32x4& cs) {
    if (c.lane < 16) {
        u32 a = L_CS + PAR * 1024 + c.hf * 512 + c.lane * 16 + c.tg * 4;
        *(LAS float*)a = cs[0];
        *(LAS float*)(a + 256) = cs[1];
    }
}
template <int PAR> DI f32x16 correct(const Ctx& c, const f32x16& acc, u32 bias) {
    f32x4 p = *(LAS f32x4*)(L_CS + PAR * 1024 + c.hf * 512 + c.t32 * 16);
    float s = (p[0] + p[1]) + (p[2] + p[3]);
    u32 hi = cvtpk(s, s) & 0xffffu;
    float r = s - lo16(hi);
    u32x4 a;
    a[0] = cvtpk(lo16(hi), r); a[1] = hi | (bias << 16); a[2] = bias; a[3] = 0;
    if (c.h) { a[0] = 0; a[1] = 0; a[2] = 0; }
    return mfma32(asbf(a), c.corrB, acc);
}
template <u32 BASE, int C0, int N> DI void issueW(const Ctx& c, const char* wb, u32 stride) {
#pragma unroll
    for (int i = 0; i < N; ++i) dma16(c.vw, wb + (size_t)(C0 + i) * 64 * stride, c.w1k, BASE + (C0 + i) * 8192);
}

DI void round0(Ctx& c, const char* xb, u32 vx0, u32 vx1, const char* wb0, const char* wb1, u32 stride, f32x16& acc, f32x4& cs) {
    u32 xa[4];
    {
        int T = 32 * c.tg + c.t32, rot = (c.tg + 2 * c.hf) & 3;
#pragma unroll
        for (int j = 0; j < 4; ++j) xa[j] = L_X + T * 128 + (((2 * ((j + rot) & 3) + c.h) ^ ((T >> 1) & 7)) << 4);
    }
    float s1 = 0.f, s2 = 0.f;
#define R0STEP(I, XOFF)                                                                       \
    {                                                                                         \
        _Pragma("unroll") for (int j = 0; j < 4; ++j) c.xf[4 * I + j] = *(LAS bf16x8*)(xa[j] + (XOFF)); \
        chunk_mma<I, L_A>(c, acc, cs);                                                        \
        _Pragma("unroll") for (int j = 0; j < 2; ++j) {                                       \
            u32x4 v = __builtin_bit_cast(u32x4, c.xf[4 * I + j]);                             \
            _Pragma("unroll") for (int e = 0; e < 4; ++e) {                                   \
                float a = lo16(v[e]), b = hi16(v[e]);                                         \
                s1 += a; s2 = __builtin_fmaf(a, a, s2); s1 += b; s2 = __builtin_fmaf(b, b, s2); \
            }                                                                                 \
        }                                                                                     \
    }
    WAITV(15); bar();
    R0STEP(0, 0)
    WAITV(12); bar();
    dma16(vx0, xb + 768, c.w2k, L_X); dma16(vx1, xb + 768, c.w2k, L_X + 1024); issueW<L_A, 6, 1>(c, wb0, stride);
    R0STEP(1, 16384)
    WAITV(12); bar();
    issueW<L_B, 2, 2>(c, wb1, stride);
    R0STEP(2, 32768)
    WAITV(11); bar();
    issueW<L_B, 4, 2>(c, wb1, stride);
    R0STEP(3, 49152)
    WAITV(10); bar();
    issueW<L_B, 6, 1>(c, wb1, stride);
    R0STEP(4, 65536)
    WAITV(8); bar();
    R0STEP(5, 81920)
    WAITV(5); bar();
    R0STEP(6, 0)
    float a, b;
    halves(s1, a, b); s1 = a + b;
    halves(s2, a, b); s2 = a + b;
    if (c.lane < 32) { f32x2 v = {s1, s2}; *(LAS f32x2*)(L_STAT + c.hf * 1024 + (32 * c.tg + c.t32) * 8) = v; }
}
DI void fin_stats(Ctx& c) {
    f32x2 u = *(LAS f32x2*)(L_STAT + (32 * c.tg + c.t32) * 8), v = *(LAS f32x2*)(L_STAT + 1024 + (32 * c.tg + c.t32) * 8);
    float m = (u[0] + v[0]) * (1.f / 448.f);
    float var = __builtin_fmaxf((u[1] + v[1]) * (1.f / 448.f) - m * m, 0.f) + 1e-5f;
    float rs = __builtin_amdgcn_rsqf(var);
    float sd = var * rs;
    c.rstd = rs;
    u32 mh = cvtpk(-m, -m);
    float ml = -m - lo16(mh);
    u32 sh = cvtpk(sd, sd);
    float sl = sd - lo16(sh);
    u32x4 b;
    b[0] = mh; b[1] = cvtpk(ml, lo16(sh)); b[2] = cvtpk(sl, 0.f); b[3] = 0;
    if (c.h) { b[0] = 0; b[1] = 0; b[2] = 0; }
    c.corrB = asbf(b);
}

DI void mish_store(const Ctx& c, const f32x16& a, char* actb, u32 voff) {
    f32x16 o;
    float rl = c.rstd * LOG2E;
#pragma unroll
    for (int r = 0; r < 16; ++r) {
        float z = a[r] * rl;
        float e = __builtin_amdgcn_exp2f(z) + 1.f;
        float q = __builtin_amdgcn_rcpf(__builtin_fmaf(e, e, 1.f));
        o[r] = z * __builtin_fmaf(q, -2.f / LOG2E, 1.f / LOG2E);
    }
    st16(voff, actb, pack8<0>(o));
    st16(voff + 512, actb, pack8<1>(o));
}

DI void hedgehog(const Ctx& c, const f32x16& t, bf16x8 wA0, bf16x8 wA1, f32x16& ep, f32x16& en) {
    f32x16 s;
#pragma unroll
    for (int r = 0; r < 16; ++r) s[r] = t[r] * c.rstd;
    f32x16 p = {};
    p = mfma32(wA0, asbf(pack8<0>(s)), p);
    p = mfma32(wA1, asbf(pack8<1>(s)), p);
    float mx = p[0], mn = p[0];
#pragma unroll
    for (int r = 1; r < 16; ++r) {
        float v = (r >= 12 && c.h) ? p[0] : p[r];
        mx = __builtin_fmaxf(mx, v); mn = __builtin_fminf(mn, v);
    }
    float a, b;
    halves(mx, a, b); mx = __builtin_fmaxf(a, b);
    halves(mn, a, b); mn = __builtin_fminf(a, b);
    float bp = -mx * LOG2E, bn = mn * LOG2E;
    float bp2 = c.h ? -__builtin_inff() : bp, bn2 = c.h ? -__builtin_inff() : bn;
    float sp = 0.f, sn = 0.f;
#pragma unroll
    for (int r = 0; r < 16; ++r) {
        ep[r] = __builtin_amdgcn_exp2f(__builtin_fmaf(p[r], LOG2E, r >= 12 ? bp2 : bp));
        en[r] = __builtin_amdgcn_exp2f(__builtin_fmaf(p[r], -LOG2E, r >= 12 ? bn2 : bn));
        sp += ep[r]; sn += en[r];
    }
    halves(sp, a, b); sp = __builtin_amdgcn_rcpf(a + b);
    halves(sn, a, b); sn = __builtin_amdgcn_rcpf(a + b);
#pragma unroll
    for (int r = 0; r < 16; ++r) { ep[r] *= sp; en[r] *= sn; }
}

template <int S0, int N, int SL0> DI void p2_issueB(const Params& P, int w, int lane, int cb, u32 wslot) {
    u32 sw = (u32)(((lane & 7) ^ ((((lane >> 4) & 3)) << 1)) << 4) + cb * 128;
#pragma unroll
    for (int i = 0; i < N; ++i) {
        constexpr int dummy = 0; (void)dummy;
        int s = S0 + i;
#pragma unroll
        for (int q = 0; q < 4; ++q) {
            if (s < 7) {
                u32 v = (u32)(lane >> 3) * 896 + sw;
                dma16(v, P.w2 + (size_t)(224 * w + 32 * s + 8 * q) * 896, wslot, (SL0 + i) * 4096 + q * 1024);
            } else {
                int row = 28 * (2 * w + (s - 7)) + 8 * q + (lane >> 3);
                row = row > 447 ? 447 : row;
                dma16((u32)row * 896 + sw, P.wo, wslot, (SL0 + i) * 4096 + q * 1024);
            }
        }
    }
}

__global__ void __launch_bounds__(512, 2) fused(Params P) {
    Ctx c;
    const int tid = threadIdx.x;
    c.lane = tid & 63; c.w = __builtin_amdgcn_readfirstlane(tid >> 6);
    c.tg = c.w & 3; c.hf = c.w >> 2; c.h = c.lane >> 5; c.t32 = c.lane & 31;
    c.w1k = c.w * 1024; c.w2k = c.w * 2048;
    const int b = blockIdx.x, xcd = b & 7, slot = b >> 3;
    const int panel = 8 * (xcd & 1) + (slot & 7), cq = xcd >> 1, kind = slot >> 3;
    const bool att = kind < 2;
    const int grp = 2 * cq + (kind & 1);
    const int lane = c.lane;
    STAMP(0);

    const u32 stride = att ? 2688u : 3584u;
    const char *wb0, *wb1, *wb2, *wb3;
    if (att) { wb0 = P.wqkv + 896 + 112 * grp; wb1 = wb0 + 896; wb2 = wb0 - 896; wb3 = wb0; }
    else { wb0 = P.w1 + 448 * grp + (grp & 1) * 64; wb1 = wb0 + 128; wb2 = wb0 + 256; wb3 = (grp & 1) ? wb0 - 128 : wb0 + 384; }
    {
        int sc = (lane & 7) ^ (((lane >> 4) & 1) << 2);
        if (att && grp == 7 && sc == 7) sc = 6;
        c.vw = (u32)(8 * c.w + (lane >> 3)) * stride + sc * 16;
    }
    const char* xb = P.x + (size_t)panel * 128 * 896;
    const u32 vx0 = (u32)(16 * c.w + (lane >> 3)) * 896 + (((lane & 7) ^ (lane >> 4)) << 4);
    const u32 vx1 = (u32)(16 * c.w + 8 + (lane >> 3)) * 896 + (((lane & 7) ^ (4 + (lane >> 4))) << 4);
    if (att) { if (tid < 196) dma16(tid * 16, P.wphi + grp * 3136, c.w1k, L_WPHI); }
    else { if (tid < 28) dma16(tid * 16, P.b1 + grp * 448, c.w1k, L_WPHI); }
#pragma unroll
    for (int i = 0; i < 6; ++i) {
        dma16(vx0, xb + 128 * i, c.w2k, L_X + 16384 * i);
        dma16(vx1, xb + 128 * i, c.w2k, L_X + 16384 * i + 1024);
        dma16(c.vw, wb0 + (size_t)i * 64 * stride, c.w1k, L_A + i * 8192);
    }
    u32 tag;
    asm volatile("s_load_dword %0, %1, 0x0\n\ts_waitcnt lgkmcnt(0)" : "=s"(tag) : "s"(P.ws) : "memory");
    tag += 1;
    {
        int g16 = lane >> 4, q = (lane & 15) >> 2, p = lane & 3, rot = (c.tg + 2 * c.hf) & 3;
        int lb = (att ? 56 : 64) * c.hf + (g16 & 1) * 32 + p * 8;
        u32 col = (((lb >> 4) ^ ((q >> 1) << 2)) << 4) | (lb & 15);
#pragma unroll
        for (int j = 0; j < 4; ++j) c.wa[j] = (u32)(16 * ((j + rot) & 3) + 8 * (g16 >> 1) + q) * 128 + col;
        u32 one = ((lane & 15) == (g16 & 1)) ? 0x3f803f80u : 0u;
        u32x4 m = {one, one, one, one};
        c.mask = asbf(m);
    }
    char* actb = P.ws + WS_ACT + (size_t)(panel * 8 + 2 * c.tg) * 72 * 1024;
    const u32 vact = (u32)(c.t32 >> 4) * 72 * 1024 + c.h * 256 + (lane & 15) * 16;

    f32x16 acc0 = {}, acc1 = {};
    f32x4 cs = {};
    round0(c, xb, vx0, vx1, wb0, wb1, stride, acc0, cs);
    cs_write<0>(c, cs);
    STAMP(1);
    WAITV(3); bar();
    issueW<L_B, 0, 2>(c, wb1, stride);
    issueW<L_A, 0, 7>(c, wb2, stride);
    fin_stats(c);

    int p2id = -1;
    if (!att) p2id = (kind - 2) * 8 + (slot & 7);
    else if (kind != (cq == 3 ? 0 : 1)) p2id = 16 + (slot & 7);
    else if ((slot & 7) < 4) p2id = 24 + (slot & 7);
    const int p2tb = p2id / 7, p2cb = p2id - 7 * p2tb;
    const u32 wslot = c.w * 20480;

    if (!att) {
        const int odd = grp & 1;
        u32 bias[4];
#pragma unroll
        for (int r = 0; r < 4; ++r) {
            int i = r < 3 ? 2 * r + c.hf + odd : (odd ? 0 : 6);
            bias[r] = *(LAS u16*)(L_WPHI + (32 * i + c.t32) * 2);
        }
        const int st0 = 9 * grp + c.hf + odd;
        cs = f32x4{};
        f32x16 e = correct<0>(c, acc0, bias[0]);
        chunk_mma<2, L_B>(c, acc1, cs); chunk_mma<3, L_B>(c, acc1, cs);
        mish_store(c, e, actb + (size_t)st0 * 1024, vact);
        WAITV(11); bar();
        chunk_mma<4, L_B>(c, acc1, cs); chunk_mma<5, L_B>(c, acc1, cs); chunk_mma<6, L_B>(c, acc1, cs);
        WAITV(9); bar();
        chunk_mma<0, L_B>(c, acc1, cs); chunk_mma<1, L_B>(c, acc1, cs);
        cs_write<1>(c, cs);
        STAMP(2);
        WAITV(2); bar();
        issueW<L_B, 0, 7>(c, wb3, stride);
        cs = f32x4{}; acc0 = f32x16{};
        e = correct<1>(c, acc1, bias[1]);
        chunk_mma<0, L_A>(c, acc0, cs); chunk_mma<1, L_A>(c, acc0, cs); chunk_mma<2, L_A>(c, acc0, cs);
        mish_store(c, e, actb + (size_t)(st0 + 2) * 1024, vact);
        chunk_mma<3, L_A>(c, acc0, cs); chunk_mma<4, L_A>(c, acc0, cs); chunk_mma<5, L_A>(c, acc0, cs); chunk_mma<6, L_A>(c, acc0, cs);
        cs_write<0>(c, cs);
        STAMP(3);
        WAITV(2); bar();
        e = correct<0>(c, acc0, bias[2]);
        const bool act3 = c.hf == odd;
        if (act3) {
            cs = f32x4{}; acc1 = f32x16{};
            chunk_mma<0, L_B>(c, acc1, cs); chunk_mma<1, L_B>(c, acc1, cs); chunk_mma<2, L_B>(c, acc1, cs);
            mish_store(c, e, actb + (size_t)(st0 + 4) * 1024, vact);
            chunk_mma<3, L_B>(c, acc1, cs); chunk_mma<4, L_B>(c, acc1, cs); chunk_mma<5, L_B>(c, acc1, cs); chunk_mma<6, L_B>(c, acc1, cs);
            cs_write<1>(c, cs);
        } else {
            mish_store(c, e, actb + (size_t)(st0 + 4) * 1024, vact);
        }
        STAMP(4);
        bar();
        if (act3) {
            e = correct<1>(c, acc1, bias[3]);
            mish_store(c, e, actb + (size_t)(9 * grp + (odd ? 0 : 6)) * 1024, vact);
        }
        p2_issueB<0, 4, 0>(P, c.w, lane, p2cb, wslot);
        WAITV(16); bar();
        if (tid == 0) stflag(P.ws + WS_AFLAG + (panel * 8 + grp) * 128, tag);
        STAMP(5);
    } else {
        const int head = 2 * grp + c.hf;
        bf16x8 wA0, wA1;
        {
            u32 v[16];
#pragma unroll
            for (int i = 0; i < 16; ++i) {
                int d = 16 * (i >> 3) + 4 * c.h + (i & 3) + 8 * ((i >> 2) & 1);
                bool ok = c.t32 < 28 && d < 28;
                u32 t = *(LAS u16*)(L_WPHI + c.hf * 1568 + (ok ? d * 56 + c.t32 * 2 : 0));
                v[i] = ok ? t : 0u;
            }
            u32x4 a0, a1;
#pragma unroll
            for (int i = 0; i < 4; ++i) { a0[i] = v[2 * i] | (v[2 * i + 1] << 16); a1[i] = v[8 + 2 * i] | (v[9 + 2 * i] << 16); }
            wA0 = asbf(a0); wA1 = asbf(a1);
        }
        cs = f32x4{};
        const int T = 32 * c.tg + c.t32;
        {
            f32x16 e = correct<0>(c, acc0, 0);
            chunk_mma<2, L_B>(c, acc1, cs); chunk_mma<3, L_B>(c, acc1, cs);
            f32x16 ep, en;
            hedgehog(c, e, wA0, wA1, ep, en);
            u32 g = (T & 1) | (((T >> 2) & 3) << 1) | (((T >> 1) & 1) << 3);
            u32 base = L_PHI + c.hf * 16384 + T * 128;
#pragma unroll
            for (int q = 0; q < 4; ++q) {
                u32x2 a = {cvtpk(ep[4 * q], ep[4 * q + 1]), cvtpk(ep[4 * q + 2], ep[4 * q + 3])};
                u32x2 n = {cvtpk(en[4 * q], en[4 * q + 1]), cvtpk(en[4 * q + 2], en[4 * q + 3])};
                *(LAS u32x2*)(base + (((2 * q + c.h) ^ g) << 3)) = a;
                *(LAS u32x2*)(base + (((8 + 2 * q + c.h) ^ g) << 3)) = n;
            }
        }
        WAITV(9); bar();
        chunk_mma<4, L_B>(c, acc1, cs); chunk_mma<5, L_B>(c, acc1, cs); chunk_mma<6, L_B>(c, acc1, cs);
        WAITV(7); bar();
        chunk_mma<0, L_B>(c, acc1, cs); chunk_mma<1, L_B>(c, acc1, cs);
        cs_write<1>(c, cs);
        STAMP(2);
        WAITV(0); bar();
        {
            f32x16 e = correct<1>(c, acc1, 0);
#pragma unroll
            for (int r = 0; r < 16; ++r) e[r] *= c.rstd;
            if (c.h) e[12] = 1.f;
            u32 base = L_V + c.hf * 8192 + T * 64, g = (T >> 1) & 7;
#pragma unroll
            for (int q = 0; q < 4; ++q) {
                u32x2 a = {cvtpk(e[4 * q], e[4 * q + 1]), cvtpk(e[4 * q + 2], e[4 * q + 3])};
                *(LAS u32x2*)(base + (((2 * q + c.h) ^ g) << 3)) = a;
            }
        }
        bar();
        if (c.tg < 2) {
            int g16 = lane >> 4, q = (lane & 15) >> 2, p = lane & 3, hh = g16 >> 1;
            u32 pa[2], va[2];
#pragma unroll
            for (int s = 0; s < 2; ++s) {
                int Tl = 8 * hh + 4 * s + q;
                u32 g = (Tl & 1) | (((Tl >> 2) & 3) << 1) | (((Tl >> 1) & 1) << 3);
                pa[s] = L_PHI + c.hf * 16384 + Tl * 128 + (((8 * c.tg + 4 * (g16 & 1) + p) ^ g) << 3);
                va[s] = L_V + c.hf * 8192 + Tl * 64 + (((4 * (g16 & 1) + p) ^ ((Tl >> 1) & 7)) << 3);
            }
            f32x16 kv = {};
#pragma unroll
            for (int ks = 0; ks < 8; ++ks)
                kv = mfma32(trd2(pa[0] + ks * 2048, pa[1] + ks * 2048), trd2(va[0] + ks * 1024, va[1] + ks * 1024), kv);
            char* kb = P.ws + WS_KVP + (size_t)((head * 16 + panel) * 4 + 2 * c.tg) * 1024;
            st16(lane * 16, kb, pack8<0>(kv));
            st16(lane * 16 + 1024, kb, pack8<1>(kv));
        }
        cs = f32x4{}; acc0 = f32x16{};
        chunk_mma<0, L_A>(c, acc0, cs); chunk_mma<1, L_A>(c, acc0, cs);
        if (c.tg < 2) {
            WAITV(0);
            if (lane == 0) stflag(P.ws + WS_KVFLAG + ((head * 2 + c.tg) * 16 + panel) * 128, tag);
        }
        chunk_mma<2, L_A>(c, acc0, cs); chunk_mma<3, L_A>(c, acc0, cs);
        chunk_mma<4, L_A>(c, acc0, cs); chunk_mma<5, L_A>(c, acc0, cs); chunk_mma<6, L_A>(c, acc0, cs);
        cs_write<0>(c, cs);
        STAMP(3);
        bar();
        poll((lane & 15) * 128, P.ws + WS_KVFLAG + (head * 2 + (c.tg >> 1)) * 2048, tag);
        STAMP(4);
        u32x4 gk[16];
        {
            const char* kb = P.ws + WS_KVP + (size_t)(head * 64 + c.tg) * 1024;
#pragma unroll
            for (int p = 0; p < 16; ++p)
                asm volatile("global_load_dwordx4 %0, %1, %2 sc1" : "=v"(gk[p]) : "v"(lane * 16), "s"(kb + p * 4096) : "memory");
        }
        if (p2id >= 0) p2_issueB<0, 4, 0>(P, c.w, lane, p2cb, wslot);
        bf16x8 fq[4];
        {
            f32x16 e = correct<0>(c, acc0, 0), ep, en;
            hedgehog(c, e, wA0, wA1, ep, en);
            fq[0] = asbf(pack8<0>(ep)); fq[1] = asbf(pack8<1>(ep)); fq[2] = asbf(pack8<0>(en)); fq[3] = asbf(pack8<1>(en));
        }
        if (p2id >= 0) WAITV(16); else WAITV(0);
        {
            float s[8] = {};
#pragma unroll
            for (int p = 0; p < 16; ++p) {
                asm volatile("" : "+v"(gk[p]));
#pragma unroll
                for (int e = 0; e < 4; ++e) { s[2 * e] += lo16(gk[p][e]); s[2 * e + 1] += hi16(gk[p][e]); }
            }
            u32x4 r = {cvtpk(s[0], s[1]), cvtpk(s[2], s[3]), cvtpk(s[4], s[5]), cvtpk(s[6], s[7])};
            *(LAS u32x4*)(16384 + c.hf * 20480 + c.tg * 1024 + lane * 16) = r;
        }
        bar();
        {
            f32x16 o = {};
#pragma unroll
            for (int s = 0; s < 4; ++s) o = mfma32(*(LAS bf16x8*)(16384 + c.hf * 20480 + s * 1024 + lane * 16), fq[s], o);
            float a, d;
            halves(o[12], a, d);
            float rd = __builtin_amdgcn_rcpf(d + 1e-6f);
#pragma unroll
            for (int r = 0; r < 16; ++r) o[r] = (r >= 12 && c.h) ? 0.f : o[r] * rd;
            char* ob = actb + (size_t)(9 * grp + 7 + c.hf) * 1024;
            st16(vact, ob, pack8<0>(o));
            st16(vact + 512, ob, pack8<1>(o));
        }
        WAITV(0); bar();
        if (tid == 0) stflag(P.ws + WS_OFLAG + (panel * 8 + grp) * 128, tag);
        STAMP(5);
        if (p2id < 0) {
            if (b == 8 * 15 && c.w == 0) {
#pragma unroll 1
                for (int i = 0; i < 4; ++i) poll((i * 64 + lane) * 128, P.ws + WS_OFLAG, tag);
                if (lane == 0) stflag(P.ws, tag);
            }
            return;
        }
    }

    const int tok0 = 256 * xcd + 64 * p2tb, p2panel = 2 * xcd + (p2tb >> 1);
    u32 ba[4];
    {
        int lg = lane >> 4, q = (lane & 15) >> 2, p = lane & 3;
        int row = 16 * (lg >> 1) + 4 * (lg & 1) + q, m = (row >> 1) & 3;
#pragma unroll
        for (int i = 0; i < 4; ++i) ba[i] = wslot + row * 128 + ((i ^ m) << 5) + p * 8;
    }
    bf16x8 br[4][4];
    WAITV(0);
#pragma unroll
    for (int s = 0; s < 4; ++s)
#pragma unroll
        for (int i = 0; i < 4; ++i) br[s][i] = trd2(ba[i] + s * 4096, ba[i] + s * 4096 + 1024);
#pragma unroll
    for (int s = 0; s < 4; ++s)
#pragma unroll
        for (int i = 0; i < 4; ++i) asm volatile("" : "+v"(br[s][i]));
    p2_issueB<4, 5, 0>(P, c.w, lane, p2cb, wslot);
    u32x4 xr;
    const u32 vo = (u32)(tok0 + (tid >> 3)) * 896 + p2cb * 128 + (tid & 7) * 16;
    asm volatile("global_load_dwordx4 %0, %1, %2" : "=v"(xr) : "v"(vo), "s"(P.x) : "memory");
    STAMP(6);
    poll(0, P.ws + WS_AFLAG + (p2panel * 8 + c.w) * 128, tag);
    STAMP(7);
    asm volatile("" : "+v"(xr));
    const __amdgpu_buffer_rsrc_t ra = __builtin_amdgcn_make_buffer_rsrc((void*)(P.ws + WS_ACT + (size_t)((tok0 >> 4) * 72 + 9 * c.w) * 1024), 0, 4 * 72 * 1024, 0x00020000);
    u32x4 af[6][4];
#define P2LOAD(BUF, S) _Pragma("unroll") for (int t = 0; t < 4; ++t) af[BUF][t] = __builtin_amdgcn_raw_buffer_load_b128(ra, lane * 16, (t * 72 + (S)) * 1024, 16);
    P2LOAD(0, 0) P2LOAD(1, 1) P2LOAD(2, 2) P2LOAD(3, 3) P2LOAD(4, 4) P2LOAD(5, 5)
    f32x4 acc[4][4] = {};
#define P2REG(BUF, S) _Pragma("unroll") for (int t = 0; t < 4; ++t) _Pragma("unroll") for (int i = 0; i < 4; ++i) acc[t][i] = mfma16(br[S][i], asbf(af[BUF][t]), acc[t][i]);
#define P2LDS(BUF, SL) { bf16x8 bb[4]; _Pragma("unroll") for (int i = 0; i < 4; ++i) bb[i] = trd2(ba[i] + (SL) * 4096, ba[i] + (SL) * 4096 + 1024); \
    _Pragma("unroll") for (int t = 0; t < 4; ++t) _Pragma("unroll") for (int i = 0; i < 4; ++i) acc[t][i] = mfma16(bb[i], asbf(af[BUF][t]), acc[t][i]); }
    P2REG(0, 0) P2LOAD(0, 6) P2REG(1, 1) P2REG(2, 2) P2REG(3, 3)
    poll(0, P.ws + WS_OFLAG + (p2panel * 8 + c.w) * 128, tag);
    STAMP(8);
    P2LOAD(1, 7) P2LOAD(2, 8)
    P2LDS(4, 0) P2LDS(5, 1) P2LDS(0, 2) P2LDS(1, 3) P2LDS(2, 4)
    STAMP(9);
    bar();
    {
        u32 base = c.w * 16384 + (lane & 15) * 256;
#pragma unroll
        for (int t = 0; t < 4; ++t)
#pragma unroll
            for (int i = 0; i < 4; ++i) *(LAS f32x4*)(base + t * 4096 + (((4 * i + (lane >> 4)) ^ (lane & 7)) << 4)) = acc[t][i];
    }
    bar();
    {
        int t = tid >> 3, cc = tid & 7;
        u32 a0 = t * 256 + (((2 * cc) ^ (t & 7)) << 4), a1 = t * 256 + (((2 * cc + 1) ^ (t & 7)) << 4);
        f32x4 s0 = {lo16(xr[0]), hi16(xr[0]), lo16(xr[1]), hi16(xr[1])}, s1 = {lo16(xr[2]), hi16(xr[2]), lo16(xr[3]), hi16(xr[3])};
#pragma unroll
        for (int w = 0; w < 8; ++w) { s0 += *(LAS f32x4*)(a0 + w * 16384); s1 += *(LAS f32x4*)(a1 + w * 16384); }
        u32x4 r = {cvtpk(s0[0], s0[1]), cvtpk(s0[2], s0[3]), cvtpk(s1[0], s1[1]), cvtpk(s1[2], s1[3])};
        *(GAS u32x4*)(P.out + vo) = r;
    }
    STAMP(10);
}

extern "C" void kernel_launch(void* const* d_in, const int* in_sizes, int n_in, void* d_out, int out_size,
                              void* d_ws, size_t ws_size, hipStream_t stream) {
    Params P;
    P.x = (const char*)d_in[0]; P.wqkv = (const char*)d_in[1]; P.wphi = (const char*)d_in[2]; P.wo = (const char*)d_in[3];
    P.w1 = (const char*)d_in[5]; P.b1 = (const char*)d_in[6]; P.w2 = (const char*)d_in[7];
    P.out = (char*)d_out; P.ws = (char*)d_ws;
    (void)hipFuncSetAttribute((const void*)fused, hipFuncAttributeMaxDynamicSharedMemorySize, LDS_BYTES);
    fused<<<256, 512, LDS_BYTES, stream>>>(P);
}
```

```cpp
#pragma clang diagnostic ignored "-Wint-to-pointer-cast"
#include <hip/hip_runtime.h>
#include <stdint.h>

typedef unsigned short u16;
typedef unsigned int u32;
typedef unsigned long long u64;
typedef __attribute__((ext_vector_type(8))) __bf16 bf16x8;
typedef __attribute__((ext_vector_type(2))) __bf16 bf16x2;
typedef __attribute__((ext_vector_type(4))) short s16x4;
typedef __attribute__((ext_vector_type(16))) float f32x16;
typedef __attribute__((ext_vector_type(4))) float f32x4;
typedef __attribute__((ext_vector_type(2))) float f32x2;
typedef __attribute__((ext_vector_type(16))) u32 u32x16;
typedef __attribute__((ext_vector_type(4))) u32 u32x4;
typedef __attribute__((ext_vector_type(2))) u32 u32x2;
#define LAS __attribute__((address_space(3)))
#define GAS __attribute__((address_space(1)))
#define DI __device__ __forceinline__

constexpr u32 WS_STAT = 4096, WS_OFLAG = 40960, WS_AFLAG = WS_OFLAG + 16384, WS_KVFLAG = 73728, WS_KV2FLAG = 90112;
constexpr u32 WS_KVS = 131072, WS_KVP = 262144, WS_ACT = 2u << 20;
constexpr u32 WS_DBG = 16u << 20;
constexpr u32 L_W2 = 98304, L_W3 = 32768, L_PHI = 49152, L_V = 81920, L_SM = 155648, L_CS = 159744;
constexpr u32 LDS_BYTES = 163840;
constexpr float LOG2E = 1.4426950408889634f;

struct Params { const char *x, *wqkv, *wphi, *wo, *w1, *b1, *w2; char* ws; char* out; };

#define STAMP(i)
#define STAMP_DECL
#define STAMP_OUT

DI u32x4 mkrs(const char* p) { u64 a = (u64)p; return u32x4{(u32)a, (u32)(a >> 32) & 0xffffu, 0xffffffffu, 0x00020000u}; }
DI void setm0(u32 a, u32 imm) { asm volatile("s_add_u32 m0, %0, %1\n\ts_nop 0" :: "s"(a), "i"(imm) : "memory", "scc"); }
template <int IMM> DI void bdma(u32 voff, u32x4 rs, u32 soff) {
    asm volatile("buffer_load_dwordx4 %0, %1, %2 offen offset:%3 lds" :: "v"(voff), "s"(rs), "s"(soff), "i"(IMM) : "memory");
}
#define WAITV(n) asm volatile("s_waitcnt vmcnt(" #n ")" ::: "memory")
template <int N> DI void waitv() { asm volatile("s_waitcnt vmcnt(%0)" :: "i"(N) : "memory"); }
DI void waitl() { asm volatile("s_waitcnt lgkmcnt(0)" ::: "memory"); }
DI void bar() { asm volatile("s_waitcnt lgkmcnt(0)\n\ts_barrier" ::: "memory"); }
DI void rawbar() { asm volatile("s_barrier" ::: "memory"); }
template <int IMM> DI void st16(u32 voff, char* gbase, u32x4 v) {
    asm volatile("global_store_dwordx4 %0, %1, %2 offset:%3 sc1\n\ts_nop 1" :: "v"(voff), "v"(v), "s"(gbase), "i"(IMM) : "memory");
}
template <int IMM> DI u32x4 ld16(u32 voff, const char* gbase) {
    u32x4 v;
    asm volatile("global_load_dwordx4 %0, %1, %2 offset:%3 sc1" : "=v"(v) : "v"(voff), "s"(gbase), "i"(IMM) : "memory");
    return v;
}
DI void stflag(char* gaddr, u32 tag) {
    u32 z = 0;
    asm volatile("s_mov_b64 exec, 1\n\tglobal_store_dword %0, %1, %2 sc1\n\ts_mov_b64 exec, -1" :: "v"(z), "v"(tag), "s"(gaddr) : "memory");
}
DI void st16one(u32 voff, char* gbase, u32x4 v) {
    asm volatile("s_mov_b64 exec, 1\n\tglobal_store_dwordx4 %0, %1, %2 sc1\n\ts_mov_b64 exec, -1\n\ts_nop 0" :: "v"(voff), "v"(v), "s"(gbase) : "memory");
}
DI void st16lo(u32 voff, char* gbase, u32x4 v) {
    asm volatile("s_mov_b32 exec_hi, 0\n\tglobal_store_dwordx4 %0, %1, %2 sc1\n\ts_mov_b32 exec_hi, -1\n\ts_nop 0" :: "v"(voff), "v"(v), "s"(gbase) : "memory");
}
DI void poll(u32 voff, const char* gbase, u32 tag) {
#pragma unroll 1
    for (u32 n = 0; n < (1u << 18); ++n) {
        u32 v;
        asm volatile("global_load_dword %0, %1, %2 sc1\n\ts_waitcnt vmcnt(0)" : "=v"(v) : "v"(voff), "s"(gbase) : "memory");
        if (__builtin_amdgcn_ballot_w64(v != tag) == 0) break;
        __builtin_amdgcn_s_sleep(1);
    }
}
DI u32 cvtpk(float lo, float hi) { f32x2 v = {lo, hi}; return __builtin_bit_cast(u32, __builtin_convertvector(v, bf16x2)); }
DI u32 cvtpk(f32x2 v) { return __builtin_bit_cast(u32, __builtin_convertvector(v, bf16x2)); }
DI s16x4 trd(u32 a) { return __builtin_amdgcn_ds_read_tr16_b64_v4i16((LAS s16x4*)a); }
DI bf16x8 trd2(u32 a0, u32 a1) {
    s16x4 lo = trd(a0), hi = trd(a1);
    return __builtin_bit_cast(bf16x8, __builtin_shufflevector(lo, hi, 0, 1, 2, 3, 4, 5, 6, 7));
}
DI f32x16 mfma32(bf16x8 a, bf16x8 b, f32x16 c) { return __builtin_amdgcn_mfma_f32_32x32x16_bf16(a, b, c, 0, 0, 0); }
DI f32x4 mfma16(bf16x8 a, bf16x8 b, f32x4 c) { return __builtin_amdgcn_mfma_f32_16x16x32_bf16(a, b, c, 0, 0, 0); }
DI bf16x8 asbf(u32x4 v) { return __builtin_bit_cast(bf16x8, v); }
DI float lo16(u32 v) { return __uint_as_float(v << 16); }
DI float hi16(u32 v) { return __uint_as_float(v & 0xffff0000u); }
DI f32x2 pfma(f32x2 a, f32x2 b, f32x2 c) { return __builtin_elementwise_fma(a, b, c); }
DI f32x2 splat(float v) { return f32x2{v, v}; }
template <int R> DI f32x2 pair(const f32x16& v) { return f32x2{v[R], v[R + 1]}; }
DI void halves(float v, float& a, float& b) {
    auto r = __builtin_amdgcn_permlane32_swap(__float_as_uint(v), __float_as_uint(v), false, false);
    a = __uint_as_float(r[0]); b = __uint_as_float(r[1]);
}
template <int C> DI float dppv(float v) { return __uint_as_float(__builtin_amdgcn_update_dpp(0, __float_as_uint(v), C, 0xf, 0xf, true)); }
DI float wsum(float v) {
    v += dppv<0xB1>(v); v += dppv<0x4E>(v); v += dppv<0x141>(v); v += dppv<0x140>(v);
    auto r = __builtin_amdgcn_permlane16_swap(__float_as_uint(v), __float_as_uint(v), false, false);
    v = __uint_as_float(r[0]) + __uint_as_float(r[1]);
    float a, b;
    halves(v, a, b);
    return a + b;
}
template <int I> struct IC { static constexpr int v = I; };
template <int B, int E, class F> DI void sfor(F&& f) { if constexpr (B < E) { f(IC<B>{}); sfor<B + 1, E>(f); } }
#define SB() __builtin_amdgcn_sched_barrier(0)
#define OPQ(x) asm("" : "+v"(x))
#define PIN(x) asm volatile("" : "+v"(x))
template <int S> DI u32x4 pack8(const f32x16& v) {
    u32x4 r;
    r[0] = cvtpk(v[8 * S + 0], v[8 * S + 1]); r[1] = cvtpk(v[8 * S + 2], v[8 * S + 3]);
    r[2] = cvtpk(v[8 * S + 4], v[8 * S + 5]); r[3] = cvtpk(v[8 * S + 6], v[8 * S + 7]);
    return r;
}

struct Ctx {
    int lane, w, tg, hf, h, t32;
    u32 wa[4];
    bf16x8 xf[28];
    bf16x8 mask;
    bf16x8 corrB;
    float rstd;
    u32 vb[4], m0a, m0b, sb2, sbo;
    u32x4 rb2, rbo;
};
template <int G> DI bf16x8 wread(const Ctx& c) { return trd2(c.wa[G & 3] + (G >> 2) * 1024, c.wa[G & 3] + (G >> 2) * 1024 + 512); }
template <bool FULL, class F> DI void round_mma(Ctx& c, f32x16& acc, f32x4& cs, F&& fill) {
    bf16x8 wf[4];
    wf[0] = wread<0>(c); wf[1] = wread<1>(c); wf[2] = wread<2>(c);
    SB();
    sfor<0, 28>([&](auto g_) {
        constexpr int g = g_.v;
        if constexpr (g + 3 < 28) wf[(g + 3) & 3] = wread<g + 3>(c);
        acc = mfma32(wf[g & 3], c.xf[g], g == 0 ? f32x16{} : acc);
        if constexpr (FULL || (g & 3) == 0) cs = mfma16(c.mask, wf[g & 3], g == 0 ? f32x4{} : cs);
        PIN(acc); PIN(cs);
        fill(g_);
        SB();
    });
}
template <int T> DI void cs_write(const Ctx& c, const f32x4& cs) {
    *(LAS float*)(L_CS + T * 1024 + c.hf * 512 + c.t32 * 16 + c.tg * 4) = cs[0];
}
template <int T> DI f32x4 cs_read(const Ctx& c) { return *(LAS f32x4*)(L_CS + T * 1024 + c.hf * 512 + c.t32 * 16); }
DI float sum4(f32x4 p) { float a = p[0] + p[1], b = p[2] + p[3]; OPQ(a); OPQ(b); return a + b; }
DI bf16x8 corr_a(const Ctx& c, float s, u32 bias) {
    u32 hi = cvtpk(s, s) & 0xffffu;
    float r = s - lo16(hi);
    u32x4 a;
    a[0] = cvtpk(lo16(hi), r); a[1] = hi | (bias << 16); a[2] = bias; a[3] = 0;
    if (c.h) { a[0] = 0; a[1] = 0; a[2] = 0; }
    return asbf(a);
}
template <int S, int Q> DI void issueB(const Ctx& c) {
    if constexpr (Q == 0) setm0(S % 3 == 0 ? c.m0a : c.m0b, S % 3 == 2 ? 32768 : 0);
    if constexpr (S < 7) bdma<Q * 1024>(c.vb[Q], c.rb2, c.sb2 + S * 28672);
    else {
        u32 v = c.vb[Q];
        if (S == 8 && Q == 3) v -= c.h * (c.w == 7 ? 4 * 896 : 0);
        bdma<Q * 1024>(v, c.rbo, c.sbo + (S - 7) * 25088);
    }
}
template <int S> DI void issueB4(const Ctx& c) { issueB<S, 0>(c); issueB<S, 1>(c); issueB<S, 2>(c); issueB<S, 3>(c); }
struct BAddr { u32 a[4], b[4]; };
template <int SL> DI void bread(const BAddr& ba, bf16x8 (&d)[4]) {
#pragma unroll
    for (int i = 0; i < 4; ++i) { u32 a = (SL == 0 ? ba.a[i] : ba.b[i]) + (SL == 2 ? 32768 : 0); d[i] = trd2(a, a + 1024); }
}
DI void bpin(bf16x8 (&d)[4]) { PIN(d[0]); PIN(d[1]); PIN(d[2]); PIN(d[3]); }

constexpr int mat(int g) { for (int e = 0; e < 16; ++e) if (5 + 4 * e / 3 == g) return e; return -1; }
struct Mish {
    f32x4 p; u32 bias; bf16x8 ca; float rl, z[3], x[2], o[2]; u32x4 pk;
    template <int G, int T> DI void step(Ctx& c, f32x16& e, u32 biasa, char* dst, u32 voff) {
        if constexpr (G == 0) { p = cs_read<T>(c); bias = *(LAS u16*)biasa; }
        if constexpr (G == 2) { ca = corr_a(c, sum4(p), bias); rl = c.rstd * LOG2E; }
        if constexpr (G == 3) e = mfma32(ca, c.corrB, e);
        constexpr int A = mat(G), B = mat(G - 1), C = mat(G - 2);
        if constexpr (C >= 0) {
            o[C & 1] = __builtin_fmaf(-z[C % 3], x[C & 1], z[C % 3]); OPQ(o[C & 1]);
            if constexpr (C & 1) pk[(C >> 1) & 3] = cvtpk(o[0], o[1]);
            if constexpr ((C & 7) == 7) st16<(C >> 3) * 512>(voff, dst, pk);
        }
        if constexpr (B >= 0) {
            float t = __builtin_fmaf(x[B & 1], 0.70710678118f, 0.70710678118f);
            x[B & 1] = __builtin_amdgcn_rcpf(__builtin_fmaf(t, t, 0.5f));
        }
        if constexpr (A >= 0) { z[A % 3] = e[A] * rl; OPQ(z[A % 3]); x[A & 1] = __builtin_amdgcn_exp2f(z[A % 3]); }
    }
};
template <int S, bool PK, class F> DI void mish8(const f32x16& e, float rl, char* dst, u32 voff, F&& hook) {
    f32x2 z[4], x[4];
    sfor<0, 4>([&](auto i_) {
        constexpr int i = i_.v;
        if constexpr (PK) z[i] = pair<8 * S + 2 * i>(e) * splat(rl);
        else { float a = e[8 * S + 2 * i] * rl, b = e[8 * S + 2 * i + 1] * rl; OPQ(a); OPQ(b); z[i] = f32x2{a, b}; }
        x[i] = f32x2{__builtin_amdgcn_exp2f(z[i][0]), __builtin_amdgcn_exp2f(z[i][1])};
    });
    SB(); hook(IC<0>{}); SB();
#pragma unroll
    for (int i = 0; i < 4; ++i) {
        f32x2 u;
        if constexpr (PK) { f32x2 t = pfma(x[i], splat(0.70710678118f), splat(0.70710678118f)); u = pfma(t, t, splat(0.5f)); }
        else {
            float a = __builtin_fmaf(x[i][0], 0.70710678118f, 0.70710678118f), b = __builtin_fmaf(x[i][1], 0.70710678118f, 0.70710678118f);
            OPQ(a); OPQ(b);
            a = __builtin_fmaf(a, a, 0.5f); b = __builtin_fmaf(b, b, 0.5f);
            OPQ(a); OPQ(b);
            u = f32x2{a, b};
        }
        x[i] = f32x2{__builtin_amdgcn_rcpf(u[0]), __builtin_amdgcn_rcpf(u[1])};
    }
    SB(); hook(IC<1>{}); SB();
    u32x4 pk;
#pragma unroll
    for (int i = 0; i < 4; ++i) {
        if constexpr (PK) pk[i] = cvtpk(pfma(-z[i], x[i], z[i]));
        else {
            float a = __builtin_fmaf(-z[i][0], x[i][0], z[i][0]), b = __builtin_fmaf(-z[i][1], x[i][1], z[i][1]);
            OPQ(a); OPQ(b);
            pk[i] = cvtpk(a, b);
        }
    }
    st16<S * 512>(voff, dst, pk);
    SB(); hook(IC<2>{}); SB();
}
template <class F> DI void hedge(const Ctx& c, const f32x16& e, bf16x8 wA0, bf16x8 wA1, f32x2 (&ep)[8], f32x2 (&en)[8], F&& hook) {
    f32x16 t = mfma32(wA0, asbf(pack8<0>(e)), f32x16{});
    t = mfma32(wA1, asbf(pack8<1>(e)), t);
    SB(); hook(IC<0>{}); SB();
    float mx = __builtin_fmaxf(t[0], t[1]), mn = __builtin_fminf(t[0], t[1]);
#pragma unroll
    for (int r = 2; r < 16; r += 2) {
        mx = __builtin_fmaxf(__builtin_fmaxf(mx, t[r]), t[r + 1]);
        mn = __builtin_fminf(__builtin_fminf(mn, t[r]), t[r + 1]);
    }
    const float sc = c.rstd * LOG2E;
    float a, b;
    halves(mx, a, b); float bp = -__builtin_fmaxf(a, b) * sc;
    halves(mn, a, b); float bn = __builtin_fminf(a, b) * sc;
    float bq = c.h ? -__builtin_inff() : bp, bm = c.h ? -__builtin_inff() : bn;
    SB(); hook(IC<1>{}); SB();
    sfor<0, 4>([&](auto q_) {
        constexpr int q = q_.v;
#pragma unroll
        for (int i = 2 * q; i < 2 * q + 2; ++i) {
            f32x2 v = {t[2 * i], t[2 * i + 1]};
            f32x2 p = pfma(v, splat(sc), splat(i < 6 ? bp : bq)), n = pfma(v, splat(-sc), splat(i < 6 ? bn : bm));
            ep[i] = f32x2{__builtin_amdgcn_exp2f(p[0]), __builtin_amdgcn_exp2f(p[1])};
            en[i] = f32x2{__builtin_amdgcn_exp2f(n[0]), __builtin_amdgcn_exp2f(n[1])};
        }
        SB(); hook(IC<2 + q>{}); SB();
    });
}

DI const char* mkp(u32 lo, u32 hi) { u64 p = ((u64)hi << 32) | lo; asm("" : "+s"(p)); return (const char*)p; }
template <int C> DI void issueW(u32 m0w, u32 vw0, u32x4 rw, u32 sw, u32 s64) {
    if constexpr ((C & 3) == 0) setm0(m0w, C * 1024);
    bdma<(C & 3) * 1024>(vw0, rw, sw + C * s64 - (C & 3) * 1024);
}
constexpr int rawait(int i) { return i < 2 ? 11 : i == 2 ? 10 : i == 6 ? 9 : 8; }

__global__ void __launch_bounds__(512, 2) fused(Params) {
    u32x16 ka; u32x2 kb;
    asm volatile("s_load_dwordx16 %0, %2, 0x0\n\ts_load_dwordx2 %1, %2, 0x40" : "=&s"(ka), "=&s"(kb) : "s"(__builtin_amdgcn_kernarg_segment_ptr()));
    Ctx c;
    int tid = threadIdx.x;
    c.lane = tid & 63; c.w = __builtin_amdgcn_readfirstlane(tid >> 6);
    c.tg = c.w & 3; c.hf = c.w >> 2; c.h = c.lane >> 5; c.t32 = c.lane & 31;
    const int b = blockIdx.x, xcd = b & 7, slot = b >> 3;
    const int panel = 8 * (xcd & 1) + (slot & 7), cq = xcd >> 1, kind = slot >> 3;
    const bool att = kind < 2;
    const int grp = 2 * cq + (kind & 1), odd = grp & 1;
    int lane = c.lane, g16 = lane >> 4, q4 = (lane & 15) >> 2, p4 = lane & 3;
    STAMP_DECL STAMP(0);

    const u32 stride = att ? 2688u : 3584u, s64 = 64 * stride;
    u32 vw0, vw1;
    {
        int sc = (lane & 7) ^ ((g16 & 1) << 2);
        if (att && grp == 7 && sc == 7) sc = 6;
        vw0 = (u32)(8 * c.w + (lane >> 3)) * stride + sc * 16 + 2048;
        vw1 = vw0 + (att ? 896 : 128) - 1024;
    }
    const u32 vx0 = (u32)(16 * c.w + (lane >> 3)) * 896 + (((lane & 7) ^ g16) << 4);
    const u32 vx1 = (u32)(16 * c.w + 8 + (lane >> 3)) * 896 + (((lane & 7) ^ (4 + g16)) << 4) - 1024;
    const int stok = 8 * ((att ? 0 : 8) + grp) + c.w;
    const u32 vrow = (u32)stok * 896 + (lane < 55 ? lane : 55) * 16;
    const u32 w4k = c.w * 4096;
    asm volatile("s_waitcnt lgkmcnt(0)" : "+s"(ka), "+s"(kb));
    const char *px = mkp(ka[0], ka[1]), *pqkv = mkp(ka[2], ka[3]), *pphi = mkp(ka[4], ka[5]), *pwo = mkp(ka[6], ka[7]);
    const char *pw1 = mkp(ka[8], ka[9]), *pb1 = mkp(ka[10], ka[11]), *pw2 = mkp(ka[12], ka[13]);
    char *ws = (char*)mkp(ka[14], ka[15]), *pout = (char*)mkp(kb[0], kb[1]);
    u32 tag;
    asm volatile("s_load_dword %0, %1, 0x0" : "=s"(tag) : "s"(ws));
    const char* xb = px + (size_t)panel * 128 * 896;
    char* pst = ws + WS_STAT + panel * 2048;
    asm("" : "+s"(pst));
    const u32x4 rx = mkrs(xb);
    const char* wbase = (att ? pqkv + 896 + 112 * grp : pw1 + 448 * grp + odd * 64) - 4096;
    const u32x4 rw = mkrs(wbase);
    u32x4 xrow;
    asm volatile("global_load_dwordx4 %0, %1, %2" : "=v"(xrow) : "v"(vrow), "s"(xb) : "memory");
    auto chunk = [&](auto i_, auto o_) {
        constexpr int I = i_.v, O = o_.v;
        if constexpr (O == 0) { setm0(w4k, (I & 3) * 32768); bdma<0>(vx0, rx, 128 * I); }
        if constexpr (O == 1) bdma<2048>(vw0, rw, I * s64);
        if constexpr (O == 2) bdma<1024>(vx1, rx, 128 * I);
        if constexpr (O == 3) bdma<3072>(vw1, rw, I * s64);
    };
    if (c.w < (att ? 4 : 1)) {
        u32 lim = att ? 3120u : 432u, v = tid * 16;
        setm0(c.w * 1024, L_SM);
        bdma<0>(v < lim ? v : lim, mkrs(att ? pphi + grp * 3136 : pb1 + grp * 448), 0);
    }
    sfor<0, 4>([&](auto o_) { chunk(IC<0>{}, o_); });
    rawbar();
    sfor<0, 4>([&](auto o_) { chunk(IC<1>{}, o_); });
    {
        asm volatile("s_waitcnt vmcnt(8) lgkmcnt(0)" : "+s"(tag), "+v"(xrow));
        tag += 1;
                float v[8], s = 0.f, s2 = 0.f;
#pragma unroll
                for (int e = 0; e < 4; ++e) { v[2 * e] = lo16(xrow[e]); v[2 * e + 1] = hi16(xrow[e]); s += v[2 * e] + v[2 * e + 1]; }
                float m = wsum(lane < 56 ? s : 0.f) * (1.f / 448.f);
#pragma unroll
                for (int e = 0; e < 8; ++e) { float d = v[e] - m; s2 = __builtin_fmaf(d, d, s2); }
                float var = wsum(lane < 56 ? s2 : 0.f) * (1.f / 448.f) + 1e-5f;
                float rs = __builtin_amdgcn_rsqf(var), sd = var * rs;
                u32 mh = cvtpk(-m, sd);
                u32 ml = cvtpk(-m - lo16(mh), sd - hi16(mh));
                st16one(stok * 16, pst, u32x4{(mh & 0xffffu) | (ml << 16), (mh >> 16) | (ml & 0xffff0000u), __float_as_uint(rs), tag});
    }
    rawbar();
    sfor<0, 4>([&](auto o_) { chunk(IC<2>{}, o_); });
    u32 pf0, pf1;
    {
        const int sl = slot & 7;
        u32 v; const char* p;
        if (c.w < 2) { v = (64 * c.w + lane) * 896 + (3 + kind) * 128; p = xb; }
        else {
            int r = c.w - 2;
            u32 row = r < 2 ? 192 + 32 * sl + (lane & 31) : 56 * sl + (lane < 56 ? lane : 55);
            u32 col = r < 2 ? r * (att ? 896 : 128) + (att ? (lane >> 5) * 96 : 0) : (att ? (r & 1) * 96 - 896 : ((r & 1) ? (odd ? -128 : 384) : 256));
            v = row * stride + col + 4096; p = wbase;
        }
        asm volatile("global_load_dword %0, %1, %2" : "=v"(pf0) : "v"(v), "s"(p) : "memory");
        u32 l = (slot * 8 + c.w) * 64 + lane;
        bool o = l >= 12544;
        l = o ? l - 12544 : l;
        l = l < 12544 - 9408 * o ? l : 0;
        asm volatile("global_load_dword %0, %1, off" : "=v"(pf1) : "v"((o ? pwo : pw2) + l * 128) : "memory");
    }
    STAMP(23); SB();

    const int rot = (c.tg + 2 * c.hf) & 3;
    int T = 32 * c.tg + c.t32;
    u32 xa[4], vst;
    {
        int lb = (att ? 56 : 64) * c.hf + (g16 & 1) * 32 + p4 * 8;
        u32 wq = q4 * 128 + ((((lb >> 4) ^ ((q4 >> 1) << 2)) << 4) | (lb & 15));
#pragma unroll
        for (int j = 0; j < 4; ++j) {
            int jj = (j + rot) & 3;
            c.wa[j] = (u32)(2 * jj + c.h) * 4096 + 2048 + wq;
            xa[j] = (u32)(T >> 4) * 4096 + (T & 15) * 128 + (((2 * jj + c.h) ^ ((T >> 1) & 7)) << 4);
        }
        u32 one = ((g16 ^ q4) & 1) ? 0u : 0x3f803f80u;
        c.mask = asbf(u32x4{one, one, one, one});
    }
    int p2id;
    if (!att) p2id = (kind - 2) * 8 + (slot & 7);
    else if (kind != (cq == 3 ? 0 : 1)) p2id = 16 + (slot & 7);
    else p2id = 24 + (slot & 7);
    const bool p2on = p2id < 28;
    const int p2tb = p2on ? p2id / 7 : 0, p2cb = p2on ? p2id - 7 * p2tb : 0;
    {
        u32 vb = (u32)(lane >> 3) * 896 + (((lane & 7) ^ ((g16 & 3) << 1)) << 4);
#pragma unroll
        for (int i = 0; i < 4; ++i) c.vb[i] = vb + i * 6144;
        c.m0a = w4k; c.m0b = w4k + (att ? 32768 : 90112);
        c.rb2 = mkrs(pw2 + p2cb * 128); c.rbo = mkrs(pwo + p2cb * 128);
        c.sb2 = c.w * 224 * 896; c.sbo = c.w * 56 * 896;
    }
    const u32 sW2 = (att ? -896 : 256) + 2048, sW3 = (odd ? -128 : 384) + 2048;
    const u32 m0w = c.w * 7168;

    f32x16 acc0, acc1, acc2;
    f32x4 cs0, cs1;
    u32x4 rec;
    {
        bf16x8 wf[12];
        sfor<0, 8>([&](auto i_) {
            constexpr int I = i_.v, J = I - 1;
            auto rdw = [&](auto n_) {
                constexpr int n = n_.v;
                constexpr u32 o = (I & 1) * 32768 + (n & 1) * 1024;
                wf[(8 * I + n) % 12] = trd2(c.wa[n >> 1] + o, c.wa[n >> 1] + o + 512);
            };
            if constexpr (I < 7) {
                if constexpr (I == 2 || I == 4 || I == 6) {
#pragma unroll
                    for (int j = 0; j < 4; ++j) {
                        xa[j] += I == 4 ? -65536 : 65536; c.wa[j] += I == 4 ? -65536 : 65536;
                        PIN(xa[j]); PIN(c.wa[j]);
                    }
                }
                waitv<rawait(I)>();
                bar();
                STAMP(11 + I);
                if constexpr (I == 3) asm volatile("" :: "v"(pf0), "v"(pf1));
                SB();
            }
            auto rdx = [&](auto j_) { c.xf[4 * I + j_.v] = *(LAS bf16x8*)(xa[j_.v] + (I & 1) * 32768); };
            if constexpr (I == 0) {
                sfor<0, 4>(rdx);
                sfor<0, 8>(rdw);
                sfor<0, 4>([&](auto o_) { chunk(IC<3>{}, o_); });
                SB();
                SB();
            } else {
                sfor<0, 8>([&](auto n_) {
                    constexpr int n = n_.v;
                    bf16x8 a = wf[(8 * J + n) % 12];
                    if constexpr (n & 1) acc1 = mfma32(a, c.xf[4 * J + (n >> 1)], (J == 0 && n == 1) ? f32x16{} : acc1);
                    else acc0 = mfma32(a, c.xf[4 * J + (n >> 1)], (J == 0 && n == 0) ? f32x16{} : acc0);
                    if constexpr (n == 0) cs0 = mfma16(c.mask, a, J == 0 ? f32x4{} : cs0);
                    if constexpr (n == 1) cs1 = mfma16(c.mask, a, J == 0 ? f32x4{} : cs1);
                    if constexpr (n & 1) PIN(acc1); else PIN(acc0);
                    if constexpr (n == 0) PIN(cs0);
                    if constexpr (n == 1) PIN(cs1);
                    if constexpr (I < 7) {
                        if constexpr (n == 0) { rdx(IC<0>{}); rdw(IC<0>{}); }
                        if constexpr (n == 1) { rdw(IC<1>{}); rdx(IC<1>{}); }
                        if constexpr (n == 2) { rdw(IC<2>{}); rdw(IC<3>{}); }
                        if constexpr (n == 3) { rdx(IC<2>{}); rdw(IC<4>{}); }
                        if constexpr (n == 4) { rdw(IC<5>{}); rdx(IC<3>{}); }
                        if constexpr (n == 5) { rdw(IC<6>{}); rdw(IC<7>{}); }
                    }
                    if constexpr ((n & 1) == 0) {
                        constexpr int o = n >> 1;
                        if constexpr (I == 5 && o == 0) { PIN(tid); vst = (tid & 31) * 16 + c.tg * 512; }
                        if constexpr (I == 5 && o == 0) rec = ld16<0>(vst, pst);
                        if constexpr (I <= 3) chunk(IC<I + 3>{}, IC<o>{});
                        if constexpr (I == 4) issueW<o>(m0w + L_W2, vw0, rw, sW2, s64);
                        if constexpr (I == 5 && o < 3) issueW<4 + o>(m0w + L_W2, vw0, rw, sW2, s64);
                        if constexpr (I == 5 && o == 3) issueB<0, 0>(c);
                        if constexpr (I == 6 && o == 0) issueB<0, 1>(c);
                        if constexpr (I == 6 && o == 2) issueB<0, 2>(c);
                        if constexpr (I == 7 && o == 0) issueB<0, 3>(c);
                    }
                    SB();
                });
            }
        });
    }
    PIN(tid);
    lane = c.lane = tid & 63; c.h = lane >> 5; c.t32 = lane & 31;
    g16 = lane >> 4; q4 = (lane & 15) >> 2; p4 = lane & 3; T = 32 * c.tg + c.t32;
    cs_write<0>(c, cs0);
    cs_write<1>(c, cs1);
#pragma unroll
    for (int j = 0; j < 4; ++j) c.wa[j] += c.h * 3072 + (((j + rot) & 3) * 6144 + L_W2 - 65536 - 2048);
    STAMP(1);
    if (att) WAITV(7); else WAITV(3);
    PIN(rec);
#pragma unroll 1
    for (u32 n = 0; n < (1u << 18) && __builtin_amdgcn_ballot_w64(rec[3] != tag) != 0; ++n) {
        rec = ld16<0>(vst, pst);
        WAITV(0);
        PIN(rec);
    }
    c.corrB = asbf(u32x4{(rec[0] & 0xffffu) | (rec[0] << 16), (rec[0] >> 16) | (rec[1] << 16), rec[1] >> 16, 0});
    c.rstd = __uint_as_float(rec[2]);
    bar();
    STAMP(2);

    char* actb = ws + WS_ACT + (size_t)((panel * 8 + 2 * c.tg) * 72 + 9 * grp) * 1024;
    const u32 vact = (u32)(c.t32 >> 4) * 72 * 1024 + c.h * 256 + (lane & 15) * 16;
    char* myflag = ws + (att ? WS_OFLAG : WS_AFLAG) + (panel * 8 + grp) * 128 + c.w * 16;
    const int tok0 = 256 * xcd + 64 * p2tb, p2panel = 2 * xcd + (p2tb >> 1);
    const u32 vfl = (u32)(p2panel * 8 + c.w) * 128 + (2 * (p2tb & 1) + (lane & 1) + 4 * ((lane >> 1) & 1)) * 16;
    const __amdgpu_buffer_rsrc_t ra = __builtin_amdgcn_make_buffer_rsrc((void*)(ws + WS_ACT + (size_t)((tok0 >> 4) * 72 + 9 * c.w) * 1024), 0, 4 * 72 * 1024, 0x00020000);
    BAddr ba;
    auto mkba = [&]() {
        int row = 16 * c.h + 4 * (g16 & 1) + q4, m = (row >> 1) & 3;
#pragma unroll
        for (int i = 0; i < 4; ++i) { u32 a = row * 128 + ((i ^ m) << 5) + p4 * 8; ba.a[i] = a + c.m0a; ba.b[i] = a + c.m0b; }
    };
    bf16x8 br[6][4];
    u32x4 af[4][4];
#define P2LOAD(BUF, S) _Pragma("unroll") for (int t = 0; t < 4; ++t) af[BUF][t] = __builtin_amdgcn_raw_buffer_load_b128(ra, lane * 16, (t * 72 + (S)) * 1024, 16);

    auto phase2 = [&](auto a_) __attribute__((always_inline)) {
    f32x4 acc[4][4] = {};
#define P2MM(BUF, BB) _Pragma("unroll") for (int t = 0; t < 4; ++t) _Pragma("unroll") for (int i = 0; i < 4; ++i) acc[t][i] = mfma16(BB[i], asbf(af[BUF][t]), acc[t][i]); SB();
    P2MM(0, br[0]) P2LOAD(0, 4) SB(); P2MM(1, br[1]) P2LOAD(1, 5) SB();
    P2MM(2, br[2])
    u32 fl;
    asm volatile("global_load_dword %0, %1, %2 sc1" : "=v"(fl) : "v"(vfl), "s"(ws + WS_OFLAG) : "memory");
    const u32 vo = (u32)(tok0 + (tid >> 3)) * 896 + p2cb * 128 + (tid & 7) * 16;
    const u32x4 xr = *(const GAS u32x4*)(px + vo);
    P2LOAD(2, 6) SB();
    P2MM(3, br[3])
    WAITV(5);
    PIN(fl);
    if (__builtin_amdgcn_ballot_w64(fl != tag) != 0) poll(vfl, ws + WS_OFLAG, tag);
    STAMP(8);
    P2LOAD(3, 7) SB();
    P2MM(0, br[4])
    P2LOAD(0, 8)
    bread<0>(ba, br[0]); bread<1>(ba, br[1]); bread<2>(ba, br[2]);
    SB();
    P2MM(1, br[5]) P2MM(2, br[0]) P2MM(3, br[1]) P2MM(0, br[2])
    STAMP(9);
    {
        const int l15 = lane & 15;
        u32 sx = ((c.w >> 1) & 1) | ((l15 >> 3) << 1) | (((l15 >> 1) & 3) << 2);
        u32 a = c.w * 8192 + (l15 >> 1) * 256 + (((l15 ^ c.w) & 1) << 7) + ((g16 ^ sx) << 3);
        u32x2 pk[4][4];
#pragma unroll
        for (int t = 0; t < 4; ++t)
#pragma unroll
            for (int i = 0; i < 4; ++i) pk[t][i] = u32x2{cvtpk(acc[t][i][0], acc[t][i][1]), cvtpk(acc[t][i][2], acc[t][i][3])};
        SB();
        bar();
#pragma unroll
        for (int t = 0; t < 4; ++t)
#pragma unroll
            for (int i = 0; i < 4; ++i) *(LAS u32x2*)((a ^ (i << 5)) + t * 2048) = pk[t][i];
    }
    {
        u32 sx = ((q4 >> 1) & 1) | ((c.w & 1) << 1) | (p4 << 2);
        u32 a = q4 * 8192 + (4 * c.w + p4) * 256 + ((q4 & 1) << 7) + (((2 * g16) ^ sx) << 3);
        bar();
        f32x4 d[2] = {};
#pragma unroll
        for (int j = 0; j < 4; ++j) {
            u32 one = (lane & 15) == 4 * j + g16 ? 0x3f803f80u : 0u;
            bf16x8 mj = asbf(u32x4{one, one, one, one});
#pragma unroll
            for (int e = 0; e < 2; ++e) { u32 aa = a ^ (e << 3) ^ (j << 6); d[e] = mfma16(trd2(aa, aa + 32768), mj, d[e]); }
        }
        constexpr float IL = 1.f / LOG2E;
        u32x4 r = {cvtpk(__builtin_fmaf(d[0][0], IL, lo16(xr[0])), __builtin_fmaf(d[0][1], IL, hi16(xr[0]))),
                   cvtpk(__builtin_fmaf(d[0][2], IL, lo16(xr[1])), __builtin_fmaf(d[0][3], IL, hi16(xr[1]))),
                   cvtpk(__builtin_fmaf(d[1][0], IL, lo16(xr[2])), __builtin_fmaf(d[1][1], IL, hi16(xr[2]))),
                   cvtpk(__builtin_fmaf(d[1][2], IL, lo16(xr[3])), __builtin_fmaf(d[1][3], IL, hi16(xr[3])))};
        *(GAS u32x4*)(pout + vo) = r;
    }
    STAMP(10);
    STAMP_OUT;
    };

    if (!att) {
        const u32 biasa = L_SM + (32 * (c.hf + odd) + c.t32) * 2;
        char* actw = actb + (c.hf + odd) * 1024;
        {
            Mish mi;
            round_mma<false>(c, acc2, cs0, [&](auto g_) {
                constexpr int g = g_.v;
                mi.step<g, 0>(c, acc0, biasa, actw, vact);
                if constexpr ((g & 1) == 0 && g < 14) issueW<g / 2>(m0w + L_W3, vw0, rw, sW3, s64);
            });
        }
        cs_write<2>(c, cs0);
        STAMP(3);
        WAITV(2);
        bar();
        const float rl = c.rstd * LOG2E;
        if (c.hf == odd) {
#pragma unroll
            for (int j = 0; j < 4; ++j) { c.wa[j] -= 65536; PIN(c.wa[j]); }
            Mish mi;
            round_mma<true>(c, acc0, cs0, [&](auto g_) {
                constexpr int g = g_.v;
                mi.step<g, 1>(c, acc1, biasa + 128, actw + 2048, vact);
                if constexpr ((g & 1) == 0 && g < 16) issueB<1 + g / 8, (g / 2) & 3>(c);
            });
            STAMP(4);
            mkba();
            f32x4 p = cs_read<2>(c);
            u32 b2 = *(LAS u16*)(biasa + 256), b3 = *(LAS u16*)(L_SM + ((odd ? 0 : 192) + c.t32) * 2);
            WAITV(2);
            bread<0>(ba, br[0]); bread<1>(ba, br[1]); bread<2>(ba, br[2]);
            acc2 = mfma32(corr_a(c, sum4(p), b2), c.corrB, acc2);
            acc0 = mfma32(corr_a(c, cs0[0], b3), c.corrB, acc0);
            bpin(br[0]); bpin(br[1]); bpin(br[2]);
            char* actd = actb + (odd ? 0 : 6144);
            mish8<0, true>(acc2, rl, actw + 4096, vact, [&](auto) {});
            mish8<1, true>(acc2, rl, actw + 4096, vact, [&](auto) {});
            mish8<0, true>(acc0, rl, actd, vact, [&](auto) {});
            mish8<1, true>(acc0, rl, actd, vact, [&](auto) {});
            STAMP(21);
            issueB4<3>(c); issueB4<4>(c); issueB4<5>(c);
            WAITV(12);
            stflag(myflag, tag);
            STAMP(22);
            WAITV(1);
            bread<0>(ba, br[3]); bread<1>(ba, br[4]); bread<2>(ba, br[5]);
            bpin(br[3]); bpin(br[4]); bpin(br[5]);
            issueB4<6>(c); issueB4<7>(c); issueB4<8>(c);
            STAMP(5);
            poll(vfl, ws + WS_AFLAG, tag);
            STAMP(7);
            P2LOAD(0, 0) P2LOAD(1, 1) P2LOAD(2, 2) P2LOAD(3, 3)
            phase2(IC<0>{});
            __builtin_amdgcn_endpgm();
        } else {
            mkba();
            f32x4 p1 = cs_read<1>(c), p2 = cs_read<2>(c);
            u32 b1 = *(LAS u16*)(biasa + 128), b2 = *(LAS u16*)(biasa + 256);
            bread<0>(ba, br[0]);
            acc1 = mfma32(corr_a(c, sum4(p1), b1), c.corrB, acc1);
            acc2 = mfma32(corr_a(c, sum4(p2), b2), c.corrB, acc2);
            bpin(br[0]);
            mish8<0, false>(acc1, rl, actw + 2048, vact, [&](auto) {});
            mish8<1, false>(acc1, rl, actw + 2048, vact, [&](auto) {});
            mish8<0, false>(acc2, rl, actw + 4096, vact, [&](auto) {});
            mish8<1, false>(acc2, rl, actw + 4096, vact, [&](auto) {});
            STAMP(4);
            issueB4<1>(c); issueB4<2>(c); issueB4<3>(c);
            WAITV(12);
            stflag(myflag, tag);
            STAMP(20);
            WAITV(1);
            bread<1>(ba, br[1]); bread<2>(ba, br[2]); bread<0>(ba, br[3]);
            bpin(br[1]); bpin(br[2]); bpin(br[3]);
            issueB4<4>(c); issueB4<5>(c); issueB4<6>(c);
            WAITV(4);
            bread<1>(ba, br[4]); bread<2>(ba, br[5]);
            bpin(br[4]); bpin(br[5]);
            issueB4<7>(c); issueB4<8>(c);
        }
        STAMP(5);
        poll(vfl, ws + WS_AFLAG, tag);
        STAMP(7);
        P2LOAD(0, 0) P2LOAD(1, 1) P2LOAD(2, 2) P2LOAD(3, 3)
        phase2(IC<0>{});
        __builtin_amdgcn_endpgm();
    } else {
        const int head = 2 * grp + c.hf;
        bf16x8 wA0, wA1;
        {
            int pc = 4 * (g16 & 1) + p4;
            u32 a = L_SM + c.hf * 1568 + (4 * c.h + q4) * 56 + (pc < 7 ? pc : 6) * 8;
            wA0 = trd2(a, a + 448);
            u32x4 t = __builtin_bit_cast(u32x4, trd2(a + 896, a + 1344));
            if (c.h) { t[2] = 0; t[3] = 0; }
            wA1 = asbf(t);
        }
        {
            f32x4 p0 = cs_read<0>(c), p1 = cs_read<1>(c);
            acc0 = mfma32(corr_a(c, sum4(p0), 0), c.corrB, acc0);
            acc1 = mfma32(corr_a(c, sum4(p1), 0), c.corrB, acc1);
            f32x2 ep[8], en[8];
            hedge(c, acc0, wA0, wA1, ep, en, [&](auto k_) {
                if constexpr (k_.v == 0) {
                    u32 base = L_V + c.hf * 8192 + T * 64, g = (T >> 1) & 7;
#pragma unroll
                    for (int q = 0; q < 4; ++q) {
                        f32x2 u = f32x2{acc1[4 * q], acc1[4 * q + 1]} * splat(c.rstd), v = f32x2{acc1[4 * q + 2], acc1[4 * q + 3]} * splat(c.rstd);
                        if (q == 3 && c.h) u[0] = 1.f;
                        *(LAS u32x2*)(base + (((2 * q + c.h) ^ g) << 3)) = u32x2{cvtpk(u), cvtpk(v)};
                    }
                }
            });
            f32x2 sp = ((ep[0] + ep[1]) + (ep[2] + ep[3])) + ((ep[4] + ep[5]) + (ep[6] + ep[7]));
            f32x2 sn = ((en[0] + en[1]) + (en[2] + en[3])) + ((en[4] + en[5]) + (en[6] + en[7]));
            float a, b2;
            halves(sp[0] + sp[1], a, b2); float rp = __builtin_amdgcn_rcpf(a + b2);
            halves(sn[0] + sn[1], a, b2); float rn = __builtin_amdgcn_rcpf(a + b2);
            const u32 pg = (T & 1) | (((T >> 2) & 3) << 1) | (((T >> 1) & 1) << 3);
            const u32 pbase = L_PHI + c.hf * 16384 + T * 128;
#pragma unroll
            for (int q = 0; q < 4; ++q) {
                *(LAS u32x2*)(pbase + (((2 * q + c.h) ^ pg) << 3)) = u32x2{cvtpk(ep[2 * q] * splat(rp)), cvtpk(ep[2 * q + 1] * splat(rp))};
                *(LAS u32x2*)(pbase + (((8 + 2 * q + c.h) ^ pg) << 3)) = u32x2{cvtpk(en[2 * q] * splat(rn)), cvtpk(en[2 * q + 1] * splat(rn))};
            }
        }
        WAITV(3);
        bar();
        STAMP(18);
        const bool kvw = (c.tg >> 1) == c.hf;
        const int sgn = c.tg & 1;
        if (kvw) {
            u32 pa[2], va[2];
#pragma unroll
            for (int s = 0; s < 2; ++s) {
                int Tl = 8 * c.h + 4 * s + q4;
                u32 g = (Tl & 1) | (((Tl >> 2) & 3) << 1) | (((Tl >> 1) & 1) << 3);
                pa[s] = L_PHI + c.hf * 16384 + Tl * 128 + (((8 * sgn + 4 * (g16 & 1) + p4) ^ g) << 3);
                va[s] = L_V + c.hf * 8192 + Tl * 64 + (((4 * (g16 & 1) + p4) ^ ((Tl >> 1) & 7)) << 3);
            }
            bf16x8 fa[8], fb[8];
#pragma unroll
            for (int ks = 0; ks < 8; ++ks) { fa[ks] = trd2(pa[0] + ks * 2048, pa[1] + ks * 2048); fb[ks] = trd2(va[0] + ks * 1024, va[1] + ks * 1024); }
            SB();
            f32x16 kv = {};
#pragma unroll
            for (int ks = 0; ks < 8; ++ks) kv = mfma32(fa[ks], fb[ks], kv);
            bar();
            char* kp = ws + WS_KVP + (size_t)((head * 16 + panel) * 4 + 2 * sgn) * 1024;
            st16<0>(lane * 16, kp, pack8<0>(kv));
            st16<1024>(lane * 16, kp, pack8<1>(kv));
        }
        u32x4 gk[8];
        const bool red = c.w == 2;
        const int rh = 2 * grp + (panel >> 3), ro = (panel & 7) * 512;
        const char* kfl = ws + WS_KVFLAG + grp * 2048 + ((panel >> 3) * 2 + ((panel >> 2) & 1)) * 16;
        auto gather = [&]() __attribute__((always_inline)) {
            const char* kp = ws + WS_KVP + rh * 65536 + ro;
#pragma unroll
            for (int i = 0; i < 8; ++i) gk[i] = ld16<0>(c.h * 4096 + c.t32 * 16, kp + i * 8192);
        };
        u32 rfl;
        bool rok = false;
        round_mma<false>(c, acc2, cs0, [&](auto g_) {
            constexpr int g = g_.v;
            if constexpr (g == 5) { if (!kvw) rawbar(); }
            if constexpr (g == 22) { if (red) asm volatile("global_load_dword %0, %1, %2 sc1" : "=v"(rfl) : "v"((lane & 15) * 128), "s"(kfl) : "memory"); }
            if constexpr (g == 27) {
                if (red) {
                    WAITV(0);
                    PIN(rfl);
                    rok = __builtin_amdgcn_ballot_w64(rfl != tag) == 0;
                    if (rok) gather();
                }
            }
            if constexpr (g == 10) {
                if (kvw) {
                    WAITV(2);
                    stflag(ws + WS_KVFLAG + (grp * 16 + panel) * 128 + (c.hf * 2 + sgn) * 16, tag);
                }
            }
            if constexpr (g >= 6 && g < 22 && (g & 1) == 0) issueB<1 + (g - 6) / 8, ((g - 6) / 2) & 3>(c);
        });
        cs_write<2>(c, cs0);
        STAMP(3);
        bar();
        mkba();
        if (red) {
            if (!rok) { poll((lane & 15) * 128, kfl, tag); gather(); }
        } else WAITV(0);
        STAMP(4);
        bread<0>(ba, br[0]); bread<1>(ba, br[1]); bread<2>(ba, br[2]);
        acc2 = mfma32(corr_a(c, sum4(cs_read<2>(c)), 0), c.corrB, acc2);
        bpin(br[0]); bpin(br[1]); bpin(br[2]);
        bf16x8 fq[4];
        {
            f32x2 ep[8], en[8];
            hedge(c, acc2, wA0, wA1, ep, en, [&](auto k_) {
                constexpr int k = k_.v, n = 2 * k;
                if constexpr (k == 0) {
                    if (red) {
                        WAITV(0);
                        f32x2 s[4] = {};
#pragma unroll
                        for (int i = 0; i < 8; ++i) {
                            PIN(gk[i]);
#pragma unroll
                            for (int e = 0; e < 4; ++e) s[e] += f32x2{lo16(gk[i][e]), hi16(gk[i][e])};
                        }
                        u32x4 r;
#pragma unroll
                        for (int e = 0; e < 4; ++e) {
                            float a0, b0, a1, b1;
                            halves(s[e][0], a0, b0); halves(s[e][1], a1, b1);
                            r[e] = cvtpk(a0 + b0, a1 + b1);
                        }
                        st16lo(lane * 16, ws + WS_KVS + rh * 4096 + ro, r);
                    }
                }
                if constexpr (k == 4) {
                    if (red) {
                        WAITV(8);
                        stflag(ws + WS_KV2FLAG + (grp * 16 + panel) * 128, tag);
                    }
                }
                issueB<3 + n / 4, n & 3>(c); issueB<3 + n / 4, (n & 3) + 1>(c);
            });
#pragma unroll
            for (int s = 0; s < 2; ++s) {
                fq[s] = asbf(u32x4{cvtpk(ep[4 * s]), cvtpk(ep[4 * s + 1]), cvtpk(ep[4 * s + 2]), cvtpk(ep[4 * s + 3])});
                fq[2 + s] = asbf(u32x4{cvtpk(en[4 * s]), cvtpk(en[4 * s + 1]), cvtpk(en[4 * s + 2]), cvtpk(en[4 * s + 3])});
            }
        }
        f32x4 sp = mfma16(c.mask, fq[1], mfma16(c.mask, fq[0], f32x4{})), sn = mfma16(c.mask, fq[3], mfma16(c.mask, fq[2], f32x4{}));
        STAMP(19);
        WAITV(0);
        bread<0>(ba, br[3]); bread<1>(ba, br[4]); bread<2>(ba, br[5]);
        float rp = __builtin_amdgcn_rcpf(sp[0]), rn = __builtin_amdgcn_rcpf(sn[0]);
        bpin(br[3]); bpin(br[4]); bpin(br[5]);
        issueB4<6>(c); issueB4<7>(c); issueB4<8>(c);
        STAMP(20);
        bool aok;
        {
            u32 va = WS_AFLAG + vfl, vk = WS_KV2FLAG + (grp * 16 + 8 * c.hf + (lane & 7)) * 128;
            OPQ(va); OPQ(vk);
            va = (lane & 8) ? va : vk;
            u64 m = 0;
#pragma unroll 1
            for (u32 n = 0; n < (1u << 18); ++n) {
                u32 v;
                asm volatile("global_load_dword %0, %1, %2 sc1\n\ts_waitcnt vmcnt(0)" : "=v"(v) : "v"(va), "s"(ws) : "memory");
                m = __builtin_amdgcn_ballot_w64(v != tag);
                if ((m & 255) == 0) break;
                __builtin_amdgcn_s_sleep(1);
            }
            aok = m == 0;
        }
        STAMP(7);
        u32x4 ks[4];
        {
            const char* kp = ws + WS_KVS + head * 4096;
            ks[0] = ld16<0>(lane * 16, kp); ks[1] = ld16<1024>(lane * 16, kp); ks[2] = ld16<2048>(lane * 16, kp); ks[3] = ld16<3072>(lane * 16, kp);
        }
        WAITV(0);
        PIN(ks[0]); PIN(ks[1]); PIN(ks[2]); PIN(ks[3]);
        {
            f32x16 np = mfma32(asbf(ks[1]), fq[1], mfma32(asbf(ks[0]), fq[0], f32x16{}));
            f32x16 nn = mfma32(asbf(ks[3]), fq[3], mfma32(asbf(ks[2]), fq[2], f32x16{}));
            float a, d;
            halves(__builtin_fmaf(np[12], rp, nn[12] * rn), a, d);
            float rd = __builtin_amdgcn_rcpf(d + 1e-6f) * LOG2E;
            rp *= rd; rn *= rd;
            u32x4 o[2];
            sfor<0, 8>([&](auto i_) {
                constexpr int i = i_.v;
                o[i >> 2][i & 3] = cvtpk(pfma(pair<2 * i>(np), splat(rp), pair<2 * i>(nn) * splat(rn)));
            });
            if (c.h) { o[1][2] = 0; o[1][3] = 0; }
            char* ob = actb + (7 + c.hf) * 1024;
            st16<0>(vact, ob, o[0]);
            st16<512>(vact, ob, o[1]);
        }
        STAMP(5);
        if (!p2on) {
            WAITV(0);
            stflag(myflag, tag);
            if (b == 8 * 15 && c.w == 0) {
#pragma unroll 1
                for (int i = 0; i < 4; ++i) poll((i * 64 + lane) * 128, ws + WS_OFLAG, tag);
                stflag(ws, tag);
            }
            STAMP_OUT;
            __builtin_amdgcn_endpgm();
        }
        if (!aok) {
            WAITV(0);
            stflag(myflag, tag);
            poll(vfl, ws + WS_AFLAG, tag);
        }
        P2LOAD(0, 0) P2LOAD(1, 1)
        WAITV(8);
        stflag(myflag, tag);
        STAMP(6);
        P2LOAD(2, 2) P2LOAD(3, 3)
        phase2(IC<1>{});
    }

}

extern "C" void kernel_launch(void* const* d_in, const int* in_sizes, int n_in, void* d_out, int out_size,
                              void* d_ws, size_t ws_size, hipStream_t stream) {
    Params P;
    P.x = (const char*)d_in[0]; P.wqkv = (const char*)d_in[1]; P.wphi = (const char*)d_in[2]; P.wo = (const char*)d_in[3];
    P.w1 = (const char*)d_in[5]; P.b1 = (const char*)d_in[6]; P.w2 = (const char*)d_in[7];
    P.out = (char*)d_out; P.ws = (char*)d_ws;
    (void)hipFuncSetAttribute((const void*)fused, hipFuncAttributeMaxDynamicSharedMemorySize, LDS_BYTES);
    fused<<<256, 512, LDS_BYTES, stream>>>(P);
}
```

```cpp
#pragma clang diagnostic ignored "-Wint-to-pointer-cast"
#include <hip/hip_runtime.h>
#include <stdint.h>

typedef unsigned short u16;
typedef unsigned int u32;
typedef unsigned long long u64;
typedef __attribute__((ext_vector_type(8))) __bf16 bf16x8;
typedef __attribute__((ext_vector_type(4))) short s16x4;
typedef __attribute__((ext_vector_type(8))) short s16x8;
typedef __attribute__((ext_vector_type(16))) float f32x16;
typedef __attribute__((ext_vector_type(4))) float f32x4;
typedef __attribute__((ext_vector_type(2))) float f32x2;
typedef __attribute__((ext_vector_type(4))) u32 u32x4;
typedef __attribute__((ext_vector_type(2))) u32 u32x2;
#define LAS __attribute__((address_space(3)))
#define GAS __attribute__((address_space(1)))
#define DI __device__ __forceinline__

constexpr u32 WS_OFLAG = 4096, WS_AFLAG = WS_OFLAG + 16384, WS_KVFLAG = 65536, WS_KVP = 131072, WS_ACT = 2u << 20;
constexpr u32 WS_DBG = 16u << 20;
constexpr u32 L_A = 0, L_B = 73728, L_X = 57344, L_PHI0 = 57344, L_PHI1 = 131072, L_V = 73728, L_WPHI = 155648, L_STAT = 159744, L_CS = 161792;
constexpr u32 LDS_BYTES = 163840;
constexpr float LOG2E = 1.4426950408889634f;

struct Params { const char *x, *wqkv, *wphi, *wo, *w1, *b1, *w2; char* out; char* ws; };

#define STAMP(i)
#define STAMP_DECL
#define STAMP_OUT

DI void dma16(u32 voff, const char* gbase, u32 ldsw, u32 imm) {
    asm volatile("s_add_u32 m0, %1, %2\n\ts_nop 0\n\tglobal_load_lds_dwordx4 %0, %3"
                 :: "v"(voff), "s"(ldsw), "i"(imm), "s"(gbase) : "memory", "scc");
}
#define WAITV(n) asm volatile("s_waitcnt vmcnt(" #n ")" ::: "memory")
DI void bar() { asm volatile("s_waitcnt lgkmcnt(0)\n\ts_barrier" ::: "memory"); }
DI void st16(u32 voff, char* gbase, u32x4 v) {
    asm volatile("global_store_dwordx4 %0, %1, %2 sc1\n\ts_nop 1" :: "v"(voff), "v"(v), "s"(gbase) : "memory");
}
DI void stflag(char* gaddr, u32 tag) {
    u32 z = 0;
    asm volatile("global_store_dword %0, %1, %2 sc1" :: "v"(z), "v"(tag), "s"(gaddr) : "memory");
}
DI void poll(u32 voff, const char* gbase, u32 tag) {
#pragma unroll 1
    for (u32 n = 0; n < (1u << 18); ++n) {
        u32 v;
        asm volatile("global_load_dword %0, %1, %2 sc1\n\ts_waitcnt vmcnt(0)" : "=v"(v) : "v"(voff), "s"(gbase) : "memory");
        if (__builtin_amdgcn_ballot_w64(v != tag) == 0) break;
        __builtin_amdgcn_s_sleep(1);
    }
}
typedef __attribute__((ext_vector_type(2))) __bf16 bf16x2;
DI u32 cvtpk(float lo, float hi) { f32x2 v = {lo, hi}; return __builtin_bit_cast(u32, __builtin_convertvector(v, bf16x2)); }
DI s16x4 trd(u32 a) { return __builtin_amdgcn_ds_read_tr16_b64_v4i16((LAS s16x4*)a); }
DI bf16x8 trd2(u32 a0, u32 a1) {
    s16x4 lo = trd(a0), hi = trd(a1);
    return __builtin_bit_cast(bf16x8, __builtin_shufflevector(lo, hi, 0, 1, 2, 3, 4, 5, 6, 7));
}
DI f32x16 mfma32(bf16x8 a, bf16x8 b, f32x16 c) { return __builtin_amdgcn_mfma_f32_32x32x16_bf16(a, b, c, 0, 0, 0); }
DI f32x4 mfma16(bf16x8 a, bf16x8 b, f32x4 c) { return __builtin_amdgcn_mfma_f32_16x16x32_bf16(a, b, c, 0, 0, 0); }
DI bf16x8 asbf(u32x4 v) { return __builtin_bit_cast(bf16x8, v); }
DI float lo16(u32 v) { return __uint_as_float(v << 16); }
DI float hi16(u32 v) { return __uint_as_float(v & 0xffff0000u); }
DI void halves(float v, float& a, float& b) {
    auto r = __builtin_amdgcn_permlane32_swap(__float_as_uint(v), __float_as_uint(v), false, false);
    a = __uint_as_float(r[0]); b = __uint_as_float(r[1]);
}
template <int I> struct IC { static constexpr int v = I; };
template <int B, int E, class F> DI void sfor(F&& f) { if constexpr (B < E) { f(IC<B>{}); sfor<B + 1, E>(f); } }
#define SB() __builtin_amdgcn_sched_barrier(0)
#define OPQ(x) asm("" : "+v"(x))
template <int S> DI u32x4 pack8(const f32x16& v) {
    u32x4 r;
    r[0] = cvtpk(v[8 * S + 0], v[8 * S + 1]); r[1] = cvtpk(v[8 * S + 2], v[8 * S + 3]);
    r[2] = cvtpk(v[8 * S + 4], v[8 * S + 5]); r[3] = cvtpk(v[8 * S + 6], v[8 * S + 7]);
    return r;
}

struct Ctx {
    int lane, w, tg, hf, h, t32;
    u32 w1k, w2k;
    u32 wa[4];
    u32 vw;
    bf16x8 xf[28];
    bf16x8 mask;
    bf16x8 corrB;
    float rstd;
};
template <int G> DI bf16x8 wread(const Ctx& c) { return trd2(c.wa[G & 3] + (G >> 2) * 8192, c.wa[G & 3] + (G >> 2) * 8192 + 512); }
DI void wa_shift(Ctx& c, int d) {
#pragma unroll
    for (int j = 0; j < 4; ++j) { c.wa[j] += d; asm volatile("" : "+v"(c.wa[j])); }
}
template <class F> DI void round_mma(Ctx& c, f32x16& acc, f32x4& cs, F&& fill) {
    bf16x8 wf[4];
    wf[0] = wread<0>(c); wf[1] = wread<1>(c); wf[2] = wread<2>(c);
    SB();
    sfor<0, 28>([&](auto g_) {
        constexpr int g = g_.v;
        if constexpr (g + 3 < 28) wf[(g + 3) & 3] = wread<g + 3>(c);
        acc = mfma32(wf[g & 3], c.xf[g], g == 0 ? f32x16{} : acc);
        if constexpr ((g & 3) == 0) cs = mfma16(c.mask, wf[g & 3], g == 0 ? f32x4{} : cs);
        fill(g_);
        SB();
    });
}
template <int PAR> DI void cs_write(const Ctx& c, const f32x4& cs) {
    if (c.lane < 16) {
        u32 a = L_CS + PAR * 1024 + c.hf * 512 + c.lane * 16 + c.tg * 4;
        *(LAS float*)a = cs[0];
        *(LAS float*)(a + 256) = cs[1];
    }
}
template <int PAR> DI f32x4 cs_read(const Ctx& c) { return *(LAS f32x4*)(L_CS + PAR * 1024 + c.hf * 512 + c.t32 * 16); }
DI bf16x8 corr_a(const Ctx& c, f32x4 p, u32 bias) {
    float s0 = p[0] + p[1], s1 = p[2] + p[3];
    OPQ(s0); OPQ(s1);
    float s = s0 + s1;
    u32 hi = cvtpk(s, s) & 0xffffu;
    float r = s - lo16(hi);
    u32x4 a;
    a[0] = cvtpk(lo16(hi), r); a[1] = hi | (bias << 16); a[2] = bias; a[3] = 0;
    if (c.h) { a[0] = 0; a[1] = 0; a[2] = 0; }
    return asbf(a);
}
template <u32 BASE, int C, bool R = false> DI void issueW(const Ctx& c, const char* wb, u32 stride) {
    dma16(c.vw, wb + (size_t)C * 64 * stride, c.w1k, BASE + C * 8192);
}

DI void round0(Ctx& c, const char* xb, u32 vx0, u32 vx1, const char* wb0, const char* wb1, u32 stride, f32x16& acc, f32x4& cs, u32* stv) {
    u32 xa[4];
    {
        int T = 32 * c.tg + c.t32, rot = (c.tg + 2 * c.hf) & 3;
#pragma unroll
        for (int j = 0; j < 4; ++j) xa[j] = L_X + T * 128 + (((2 * ((j + rot) & 3) + c.h) ^ ((T >> 1) & 7)) << 4);
    }
    float s1 = 0.f, s2 = 0.f;
    bf16x8 wf[2][4];
    sfor<0, 8>([&](auto i_) {
        constexpr int I = i_.v;
        if constexpr (I == 0) WAITV(15);
        if constexpr (I == 1 || I == 2) WAITV(12);
        if constexpr (I == 3) WAITV(11);
        if constexpr (I == 4) WAITV(10);
        if constexpr (I == 5) WAITV(9);
        if constexpr (I == 6) WAITV(7);
        if constexpr (I < 7) {
            bar();
            STAMP(11 + I);
            if constexpr (I == 4 || I == 6) {
#pragma unroll
                for (int j = 0; j < 4; ++j) { xa[j] += I == 4 ? 65536 : -65536; asm volatile("" : "+v"(xa[j])); }
            }
#pragma unroll
            for (int j = 0; j < 4; ++j) c.xf[4 * I + j] = *(LAS bf16x8*)(xa[j] + (I == 6 ? 0 : (I & 3) * 16384));
            wf[I & 1][0] = wread<4 * I>(c); wf[I & 1][1] = wread<4 * I + 1>(c); wf[I & 1][2] = wread<4 * I + 2>(c); wf[I & 1][3] = wread<4 * I + 3>(c);
            SB();
        }
        if constexpr (I > 0) {
            constexpr int J = I - 1;
            sfor<0, 4>([&](auto j_) {
                constexpr int j = j_.v;
                acc = mfma32(wf[J & 1][j], c.xf[4 * J + j], (J == 0 && j == 0) ? f32x16{} : acc);
                if constexpr (j == 0) cs = mfma16(c.mask, wf[J & 1][0], J == 0 ? f32x4{} : cs);
                u32x4 v = __builtin_bit_cast(u32x4, c.xf[4 * J + (j >> 1)]);
#pragma unroll
                for (int e = 2 * (j & 1); e < 2 * (j & 1) + 2; ++e) {
                    float a = lo16(v[e]), b = hi16(v[e]);
                    s1 += a; s2 = __builtin_fmaf(a, a, s2); OPQ(s1); s1 += b; s2 = __builtin_fmaf(b, b, s2); OPQ(s1);
                }
                if constexpr (I == 1 && j == 0) dma16(vx0, xb + 768, c.w2k, L_X);
                if constexpr (I == 1 && j == 1) dma16(vx1, xb + 768, c.w2k, L_X + 1024);
                if constexpr (I == 1 && j == 2) issueW<L_A, 6>(c, wb0, stride);
                if constexpr (I >= 2 && I <= 5 && j == 0) issueW<L_B, 2 * (I - 2)>(c, wb1, stride);
                if constexpr (I >= 2 && I <= 4 && j == 2) issueW<L_B, 2 * (I - 2) + 1>(c, wb1, stride);
                SB();
            });
        }
    });
    float a, b;
    halves(s1, a, b); s1 = a + b;
    halves(s2, a, b); s2 = a + b;
    if (c.lane < 32) { f32x2 v = {s1, s2}; *(LAS f32x2*)(L_STAT + c.hf * 1024 + (32 * c.tg + c.t32) * 8) = v; }
}
DI void fin_stats(Ctx& c, f32x2 u, f32x2 v) {
    float m = (u[0] + v[0]) * (1.f / 448.f);
    float var = __builtin_fmaxf((u[1] + v[1]) * (1.f / 448.f) - m * m, 0.f) + 1e-5f;
    float rs = __builtin_amdgcn_rsqf(var);
    float sd = var * rs;
    c.rstd = rs;
    u32 mh = cvtpk(-m, -m);
    float ml = -m - lo16(mh);
    u32 sh = cvtpk(sd, sd);
    float sl = sd - lo16(sh);
    u32x4 b;
    b[0] = mh; b[1] = cvtpk(ml, lo16(sh)); b[2] = cvtpk(sl, 0.f); b[3] = 0;
    if (c.h) { b[0] = 0; b[1] = 0; b[2] = 0; }
    c.corrB = asbf(b);
}

struct Mish {
    f32x4 p; bf16x8 ca; f32x16 e; float rl, z[3], x[2], o[2]; u32x4 pk;
    template <int G, int PAR> DI void step(Ctx& c, const f32x16& acc, u32 bias, char* dst, u32 voff) {
        if constexpr (G == 0) p = cs_read<PAR>(c);
        if constexpr (G == 2) { ca = corr_a(c, p, bias); rl = c.rstd * LOG2E; }
        if constexpr (G == 3) e = mfma32(ca, c.corrB, acc);
        constexpr int A = G - 5, B = G - 6, C = G - 7;
        if constexpr (C >= 0 && C < 16) {
            o[C & 1] = __builtin_fmaf(-z[C % 3], x[C & 1], z[C % 3]); OPQ(o[C & 1]);
            if constexpr (C & 1) pk[(C >> 1) & 3] = cvtpk(o[0], o[1]);
            if constexpr ((C & 7) == 7) st16(voff + (C >> 3) * 512, dst, pk);
        }
        if constexpr (B >= 0 && B < 16) {
            float t = __builtin_fmaf(x[B & 1], 0.70710678118f, 0.70710678118f);
            x[B & 1] = __builtin_amdgcn_rcpf(__builtin_fmaf(t, t, 0.5f));
        }
        if constexpr (A >= 0 && A < 16) { z[A % 3] = e[A] * rl; OPQ(z[A % 3]); x[A & 1] = __builtin_amdgcn_exp2f(z[A % 3]); }
    }
};

struct Hedge {
    f32x4 p; bf16x8 ca; f32x16 t, ep, en; u32x4 k0, k1; float mx, mn, sc, bp, bn, sp, sn;
    template <int G, int PAR> DI void step(Ctx& c, const f32x16& acc, bf16x8 wA0, bf16x8 wA1) {
        if constexpr (G == 0) p = cs_read<PAR>(c);
        if constexpr (G == 2) { ca = corr_a(c, p, 0); sc = c.rstd * LOG2E; }
        if constexpr (G == 3) t = mfma32(ca, c.corrB, acc);
        if constexpr (G == 5) { k0 = pack8<0>(t); k1 = pack8<1>(t); }
        if constexpr (G == 6) { t = mfma32(wA0, asbf(k0), f32x16{}); t = mfma32(wA1, asbf(k1), t); }
        if constexpr (G == 9) {
            mx = t[0]; mn = t[0];
#pragma unroll
            for (int r = 1; r < 9; ++r) { mx = __builtin_fmaxf(mx, t[r]); mn = __builtin_fminf(mn, t[r]); }
        }
        if constexpr (G == 10) {
#pragma unroll
            for (int r = 9; r < 16; ++r) { float v = (r >= 12 && c.h) ? t[0] : t[r]; mx = __builtin_fmaxf(mx, v); mn = __builtin_fminf(mn, v); }
        }
        if constexpr (G == 11) {
            float a, b;
            halves(mx, a, b); bp = -__builtin_fmaxf(a, b) * sc;
            halves(mn, a, b); bn = __builtin_fminf(a, b) * sc;
            sp = 0.f; sn = 0.f;
        }
        if constexpr (G >= 12 && G < 21) {
            constexpr int r0 = 2 * (G - 12);
            if constexpr (G == 18) { bp = c.h ? -__builtin_inff() : bp; bn = c.h ? -__builtin_inff() : bn; }
            if constexpr (G > 12) { sp += ep[r0 - 2]; OPQ(sp); sn += en[r0 - 2]; OPQ(sn); sp += ep[r0 - 1]; OPQ(sp); sn += en[r0 - 1]; OPQ(sn); }
            if constexpr (G < 20) {
#pragma unroll
                for (int r = r0; r < r0 + 2; ++r) {
                    float a = __builtin_fmaf(t[r], sc, bp), b = __builtin_fmaf(t[r], -sc, bn);
                    OPQ(a); OPQ(b);
                    ep[r] = __builtin_amdgcn_exp2f(a); en[r] = __builtin_amdgcn_exp2f(b);
                }
            }
        }
        if constexpr (G == 21) {
            float a, b;
            halves(sp, a, b); sp = __builtin_amdgcn_rcpf(a + b);
            halves(sn, a, b); sn = __builtin_amdgcn_rcpf(a + b);
        }
        if constexpr (G >= 22 && G < 26) {
#pragma unroll
            for (int r = 4 * (G - 22); r < 4 * (G - 22) + 4; ++r) { float a = ep[r] * sp, b = en[r] * sn; OPQ(a); OPQ(b); ep[r] = a; en[r] = b; }
        }
    }
};

template <int S, int Q> DI void p2_issueB1(const Params& P, int w, int lane, u32 vb, u32 slot) {
    if constexpr (S < 7) dma16(vb, P.w2 + (size_t)(224 * w + 32 * S + 8 * Q) * 896, slot, Q * 1024);
    else {
        int row0 = 28 * (2 * w + (S - 7)) + 8 * Q;
        u32 v = vb;
        if (S == 8 && Q == 3) { if (w == 7 && lane >= 32) v -= 4 * 896; }
        dma16(v, P.wo + (size_t)row0 * 896, slot, Q * 1024);
    }
}
template <int S> DI void p2_issueB(const Params& P, int w, int lane, u32 vb, u32 slot) {
    p2_issueB1<S, 0>(P, w, lane, vb, slot); p2_issueB1<S, 1>(P, w, lane, vb, slot);
    p2_issueB1<S, 2>(P, w, lane, vb, slot); p2_issueB1<S, 3>(P, w, lane, vb, slot);
}

DI void rawbar() { asm volatile("s_barrier" ::: "memory"); }
constexpr int wgap(int g) { return g == 0 ? 0 : g == 2 ? 1 : g == 5 ? 2 : g == 7 ? 3 : g == 10 ? 4 : g == 12 ? 5 : g == 15 ? 6 : -1; }

__global__ void __launch_bounds__(512, 2) fused(Params P) {
    Ctx c;
    const int tid = threadIdx.x;
    c.lane = tid & 63; c.w = __builtin_amdgcn_readfirstlane(tid >> 6);
    c.tg = c.w & 3; c.hf = c.w >> 2; c.h = c.lane >> 5; c.t32 = c.lane & 31;
    c.w1k = c.w * 1024; c.w2k = c.w * 2048;
    const int b = blockIdx.x, xcd = b & 7, slot = b >> 3;
    const int panel = 8 * (xcd & 1) + (slot & 7), cq = xcd >> 1, kind = slot >> 3;
    const bool att = kind < 2;
    const int grp = 2 * cq + (kind & 1);
    const int lane = c.lane;
    STAMP_DECL STAMP(0);

    const u32 stride = att ? 2688u : 3584u;
    const char *wb0, *wb1, *wb2, *wb3;
    if (att) { wb0 = P.wqkv + 896 + 112 * grp; wb1 = wb0 + 896; wb2 = wb0 - 896; wb3 = wb0; }
    else { wb0 = P.w1 + 448 * grp + (grp & 1) * 64; wb1 = wb0 + 128; wb2 = wb0 + 256; wb3 = (grp & 1) ? wb0 - 128 : wb0 + 384; }
    {
        int sc = (lane & 7) ^ (((lane >> 4) & 1) << 2);
        if (att && grp == 7 && sc == 7) sc = 6;
        c.vw = (u32)(8 * c.w + (lane >> 3)) * stride + sc * 16;
    }
    const char* xb = P.x + (size_t)panel * 128 * 896;
    const u32 vx0 = (u32)(16 * c.w + (lane >> 3)) * 896 + (((lane & 7) ^ (lane >> 4)) << 4);
    const u32 vx1 = (u32)(16 * c.w + 8 + (lane >> 3)) * 896 + (((lane & 7) ^ (4 + (lane >> 4))) << 4);
    sfor<0, 6>([&](auto i_) {
        constexpr int i = i_.v;
        dma16(vx0, xb + 128 * i, c.w2k, L_X + 16384 * i);
        dma16(vx1, xb + 128 * i, c.w2k, L_X + 16384 * i + 1024);
        issueW<L_A, i>(c, wb0, stride);
        if constexpr (i < 5) rawbar();
    });
    if (att) { if (tid < 196) dma16(tid * 16, P.wphi + grp * 3136, c.w1k, L_WPHI); }
    else { if (tid < 28) dma16(tid * 16, P.b1 + grp * 448, c.w1k, L_WPHI); }
    STAMP(23); SB();
    u32 tag;
    asm volatile("s_load_dword %0, %1, 0x0\n\ts_waitcnt lgkmcnt(0)" : "=s"(tag) : "s"(P.ws) : "memory");
    tag += 1;
    {
        int g16 = lane >> 4, q = (lane & 15) >> 2, p = lane & 3, rot = (c.tg + 2 * c.hf) & 3;
        int lb = (att ? 56 : 64) * c.hf + (g16 & 1) * 32 + p * 8;
        u32 col = (((lb >> 4) ^ ((q >> 1) << 2)) << 4) | (lb & 15);
#pragma unroll
        for (int j = 0; j < 4; ++j) c.wa[j] = (u32)(16 * ((j + rot) & 3) + 8 * (g16 >> 1) + q) * 128 + col;
        u32 one = ((lane & 15) == (g16 & 1)) ? 0x3f803f80u : 0u;
        u32x4 m = {one, one, one, one};
        c.mask = asbf(m);
    }
    char* actb = P.ws + WS_ACT + (size_t)(panel * 8 + 2 * c.tg) * 72 * 1024;
    const u32 vact = (u32)(c.t32 >> 4) * 72 * 1024 + c.h * 256 + (lane & 15) * 16;

    int p2id;
    if (!att) p2id = (kind - 2) * 8 + (slot & 7);
    else if (kind != (cq == 3 ? 0 : 1)) p2id = 16 + (slot & 7);
    else p2id = 24 + (slot & 7);
    const bool p2on = p2id < 28;
    const int p2tb = p2on ? p2id / 7 : 0, p2cb = p2on ? p2id - 7 * p2tb : 0;
    const u32 vb = (u32)(lane >> 3) * 896 + (((lane & 7) ^ (((lane >> 4) & 3) << 1)) << 4) + p2cb * 128;
    u32 sl[5];
    if (att) { sl[0] = 14 + c.w; sl[1] = 22 + c.w; sl[2] = 30 + c.w; sl[3] = c.w; sl[4] = c.w < 6 ? 8 + c.w : 32 + c.w; }
    else { sl[0] = c.w < 4 ? 14 + c.w : 28 + c.w; sl[1] = c.w; sl[2] = c.w < 6 ? 8 + c.w : 30 + c.w; sl[3] = 18 + c.w; sl[4] = c.w < 6 ? 26 + c.w : 32 + c.w; }
#pragma unroll
    for (int i = 0; i < 5; ++i) sl[i] *= 4096;
    char* myflag = P.ws + (att ? WS_OFLAG : WS_AFLAG) + (panel * 8 + grp) * 128 + c.w * 16;
    u32 ba[4];
    {
        int lg = lane >> 4, q = (lane & 15) >> 2, p = lane & 3;
        int row = 16 * (lg >> 1) + 4 * (lg & 1) + q, m = (row >> 1) & 3;
#pragma unroll
        for (int i = 0; i < 4; ++i) ba[i] = row * 128 + ((i ^ m) << 5) + p * 8;
    }
    bf16x8 br[4][4];

    f32x16 acc0, acc1;
    f32x4 cs;
    u32* stv = nullptr;
    round0(c, xb, vx0, vx1, wb0, wb1, stride, acc0, cs, stv);
    cs_write<0>(c, cs);
    STAMP(1);
    WAITV(0); bar();
    wa_shift(c, L_B);
    const f32x2 su = *(LAS f32x2*)(L_STAT + (32 * c.tg + c.t32) * 8), sv = *(LAS f32x2*)(L_STAT + 1024 + (32 * c.tg + c.t32) * 8);

    if (!att) {
        const int odd = grp & 1;
        u32 bias[4];
#pragma unroll
        for (int r = 0; r < 4; ++r) {
            int i = r < 3 ? 2 * r + c.hf + odd : (odd ? 0 : 6);
            bias[r] = *(LAS u16*)(L_WPHI + (32 * i + c.t32) * 2);
        }
        char* actw = actb + (size_t)(9 * grp + c.hf + odd) * 1024;
        {
            Mish mi;
            round_mma(c, acc1, cs, [&](auto g_) {
                constexpr int g = g_.v;
                if constexpr (g == 1) fin_stats(c, su, sv);
                mi.step<g, 0>(c, acc0, bias[0], actw, vact);
                if constexpr (wgap(g) >= 0) issueW<L_A, wgap(g), true>(c, wb2, stride);
                if constexpr (g == 17 || g == 20 || g == 23 || g == 25) p2_issueB1<0, g == 17 ? 0 : g == 20 ? 1 : g == 23 ? 2 : 3>(P, c.w, lane, vb, sl[0]);
            });
        }
        cs_write<1>(c, cs);
        STAMP(2);
        WAITV(5); bar();
        wa_shift(c, -(int)L_B);
        {
            Mish mi;
            round_mma(c, acc0, cs, [&](auto g_) {
                constexpr int g = g_.v;
                mi.step<g, 1>(c, acc1, bias[1], actw + 2048, vact);
                if constexpr (wgap(g) >= 0) issueW<L_B, wgap(g), true>(c, wb3, stride);
            });
        }
        cs_write<0>(c, cs);
        STAMP(3);
        WAITV(1); bar();
        wa_shift(c, L_B);
        const bool act3 = c.hf == odd;
        if (act3) {
            Mish mi;
            round_mma(c, acc1, cs, [&](auto g_) {
                constexpr int g = g_.v;
                mi.step<g, 0>(c, acc0, bias[2], actw + 4096, vact);
                if constexpr (g % 3 == 0 && g < 24) p2_issueB1<1 + g / 12, (g / 3) & 3>(P, c.w, lane, vb, sl[1 + g / 12]);
            });
            cs_write<1>(c, cs);
        } else {
            Mish mi;
            sfor<0, 23>([&](auto g_) { mi.step<g_.v, 0>(c, acc0, bias[2], actw + 4096, vact); });
            p2_issueB<1>(P, c.w, lane, vb, sl[1]);
            p2_issueB<2>(P, c.w, lane, vb, sl[2]);
            WAITV(8);
            if (lane == 0) stflag(myflag, tag);
        }
        STAMP(4);
        bar();
        Mish mi;
        mi.step<0, 1>(c, acc1, 0, nullptr, 0);
        bar();
        p2_issueB<3>(P, c.w, lane, vb, sl[3]);
        p2_issueB<8>(P, c.w, lane, vb, sl[4]);
        if (act3) {
            sfor<1, 23>([&](auto g_) { mi.step<g_.v, 1>(c, acc1, bias[3], actb + (size_t)(9 * grp + (odd ? 0 : 6)) * 1024, vact); });
            WAITV(10);
        } else WAITV(8);
        sfor<0, 3>([&](auto s_) {
            constexpr int s = s_.v;
#pragma unroll
            for (int i = 0; i < 4; ++i) br[s][i] = trd2(ba[i] + sl[s], ba[i] + sl[s] + 1024);
        });
        sfor<0, 3>([&](auto s_) {
            constexpr int s = s_.v;
#pragma unroll
            for (int i = 0; i < 4; ++i) asm volatile("" : "+v"(br[s][i]));
            p2_issueB<4 + s>(P, c.w, lane, vb, sl[s]);
        });
        WAITV(12);
        if (act3 && lane == 0) stflag(myflag, tag);
        STAMP(5);
    } else {
        const int head = 2 * grp + c.hf;
        const int T = 32 * c.tg + c.t32;
        bf16x8 wA0, wA1;
        u32 wv[16];
#pragma unroll
        for (int i = 0; i < 16; ++i) {
            int d = 16 * (i >> 3) + 4 * c.h + (i & 3) + 8 * ((i >> 2) & 1);
            bool ok = c.t32 < 28 && d < 28;
            wv[i] = *(LAS u16*)(L_WPHI + c.hf * 1568 + (ok ? d * 56 + c.t32 * 2 : 0));
        }
        {
            Hedge hd;
            const u32 pg = (T & 1) | (((T >> 2) & 3) << 1) | (((T >> 1) & 1) << 3);
            const u32 pbase = (c.hf ? L_PHI1 : L_PHI0) + T * 128;
            round_mma(c, acc1, cs, [&](auto g_) {
                constexpr int g = g_.v;
                if constexpr (g == 1) fin_stats(c, su, sv);
                if constexpr (g == 4) {
                    u32x4 a0, a1;
#pragma unroll
                    for (int i = 0; i < 4; ++i) { a0[i] = wv[2 * i] | (wv[2 * i + 1] << 16); a1[i] = wv[8 + 2 * i] | (wv[9 + 2 * i] << 16); }
                    if (c.h) { a1[2] = 0; a1[3] = 0; }
                    if (c.t32 >= 28) { a0 = u32x4{}; a1 = u32x4{}; }
                    wA0 = asbf(a0); wA1 = asbf(a1);
                }
                hd.step<g, 0>(c, acc0, wA0, wA1);
                if constexpr (g >= 23 && g < 27) {
                    constexpr int q = g - 23;
                    u32x2 a = {cvtpk(hd.ep[4 * q], hd.ep[4 * q + 1]), cvtpk(hd.ep[4 * q + 2], hd.ep[4 * q + 3])};
                    u32x2 n = {cvtpk(hd.en[4 * q], hd.en[4 * q + 1]), cvtpk(hd.en[4 * q + 2], hd.en[4 * q + 3])};
                    *(LAS u32x2*)(pbase + (((2 * q + c.h) ^ pg) << 3)) = a;
                    *(LAS u32x2*)(pbase + (((8 + 2 * q + c.h) ^ pg) << 3)) = n;
                }
                if constexpr (wgap(g) >= 0) issueW<L_A, wgap(g), true>(c, wb2, stride);
            });
        }
        cs_write<1>(c, cs);
        STAMP(2);
        bar();
        wa_shift(c, -(int)L_B);
        {
            f32x16 e = mfma32(corr_a(c, cs_read<1>(c), 0), c.corrB, acc1);
#pragma unroll
            for (int r = 0; r < 16; ++r) { float t = e[r] * c.rstd; OPQ(t); e[r] = t; }
            if (c.h) e[12] = 1.f;
            u32 base = L_V + c.hf * 8192 + T * 64, g = (T >> 1) & 7;
#pragma unroll
            for (int q = 0; q < 4; ++q) {
                u32x2 a = {cvtpk(e[4 * q], e[4 * q + 1]), cvtpk(e[4 * q + 2], e[4 * q + 3])};
                *(LAS u32x2*)(base + (((2 * q + c.h) ^ g) << 3)) = a;
            }
        }
        WAITV(0); bar();
        STAMP(18);
        const bool kvw = (c.tg >> 1) == c.hf;
        const int sgn = c.tg & 1;
        if (kvw) {
            int g16 = lane >> 4, q = (lane & 15) >> 2, p = lane & 3, hh = g16 >> 1;
            u32 pa[2], va[2];
#pragma unroll
            for (int s = 0; s < 2; ++s) {
                int Tl = 8 * hh + 4 * s + q;
                u32 g = (Tl & 1) | (((Tl >> 2) & 3) << 1) | (((Tl >> 1) & 1) << 3);
                pa[s] = (c.hf ? L_PHI1 : L_PHI0) + Tl * 128 + (((8 * sgn + 4 * (g16 & 1) + p) ^ g) << 3);
                va[s] = L_V + c.hf * 8192 + Tl * 64 + (((4 * (g16 & 1) + p) ^ ((Tl >> 1) & 7)) << 3);
            }
            bf16x8 fa[8], fb[8];
#pragma unroll
            for (int ks = 0; ks < 8; ++ks) { fa[ks] = trd2(pa[0] + ks * 2048, pa[1] + ks * 2048); fb[ks] = trd2(va[0] + ks * 1024, va[1] + ks * 1024); }
            SB();
            f32x16 kv = {};
#pragma unroll
            for (int ks = 0; ks < 8; ++ks) kv = mfma32(fa[ks], fb[ks], kv);
            bar();
            char* kb = P.ws + WS_KVP + (size_t)((head * 16 + panel) * 4 + 2 * sgn) * 1024;
            st16(lane * 16, kb, pack8<0>(kv));
            st16(lane * 16 + 1024, kb, pack8<1>(kv));
        }
        round_mma(c, acc0, cs, [&](auto g_) {
            constexpr int g = g_.v;
            if constexpr (g == 5) { if (!kvw) rawbar(); }
            if constexpr (g >= 6 && g < 24 && g % 3 != 2) {
                constexpr int n = (g - 6) / 3 * 2 + (g % 3);
                p2_issueB1<n / 4, n & 3>(P, c.w, lane, vb, sl[n / 4]);
            }
            if constexpr (g == 17) {
                if (kvw) {
                    WAITV(8);
                    if (lane == 0) stflag(P.ws + WS_KVFLAG + ((head * 2 + sgn) * 16 + panel) * 128, tag);
                }
            }
        });
        cs_write<0>(c, cs);
        STAMP(3);
        bar();
        poll((lane & 15) * 128, P.ws + WS_KVFLAG + (head * 2 + (c.tg >> 1)) * 2048, tag);
        STAMP(4);
        u32x4 gk[16];
        {
            const char* kb = P.ws + WS_KVP + (size_t)(head * 64 + c.tg) * 1024;
#pragma unroll
            for (int p = 0; p < 16; ++p)
                asm volatile("global_load_dwordx4 %0, %1, %2 sc1" : "=v"(gk[p]) : "v"(lane * 16), "s"(kb + p * 4096) : "memory");
        }
        p2_issueB<3>(P, c.w, lane, vb, sl[3]);
        sfor<0, 3>([&](auto s_) {
            constexpr int s = s_.v;
#pragma unroll
            for (int i = 0; i < 4; ++i) br[s][i] = trd2(ba[i] + sl[s], ba[i] + sl[s] + 1024);
        });
        sfor<0, 3>([&](auto s_) {
            constexpr int s = s_.v;
#pragma unroll
            for (int i = 0; i < 4; ++i) asm volatile("" : "+v"(br[s][i]));
            p2_issueB<4 + s>(P, c.w, lane, vb, sl[s]);
        });
        bf16x8 fq[4];
        {
            Hedge hd;
            sfor<0, 26>([&](auto g_) { hd.step<g_.v, 0>(c, acc0, wA0, wA1); });
            fq[0] = asbf(pack8<0>(hd.ep)); fq[1] = asbf(pack8<1>(hd.ep)); fq[2] = asbf(pack8<0>(hd.en)); fq[3] = asbf(pack8<1>(hd.en));
        }
        STAMP(19);
        WAITV(16);
        STAMP(20);
        {
            float s[8] = {};
#pragma unroll
            for (int p = 0; p < 16; ++p) {
                asm volatile("" : "+v"(gk[p]));
#pragma unroll
                for (int e = 0; e < 4; ++e) { s[2 * e] += lo16(gk[p][e]); s[2 * e + 1] += hi16(gk[p][e]); }
            }
            u32x4 r = {cvtpk(s[0], s[1]), cvtpk(s[2], s[3]), cvtpk(s[4], s[5]), cvtpk(s[6], s[7])};
            *(LAS u32x4*)(32768 + c.hf * 4096 + c.tg * 1024 + lane * 16) = r;
        }
        bar();
        {
            bf16x8 ks[4];
#pragma unroll
            for (int s = 0; s < 4; ++s) ks[s] = *(LAS bf16x8*)(32768 + c.hf * 4096 + s * 1024 + lane * 16);
            bar();
            f32x16 o = {};
#pragma unroll
            for (int s = 0; s < 4; ++s) o = mfma32(ks[s], fq[s], o);
            float a, d;
            halves(o[12], a, d);
            float rd = __builtin_amdgcn_rcpf(d + 1e-6f) * LOG2E;
#pragma unroll
            for (int r = 0; r < 16; ++r) o[r] = (r >= 12 && c.h) ? 0.f : o[r] * rd;
            char* ob = actb + (size_t)(9 * grp + 7 + c.hf) * 1024;
            st16(vact, ob, pack8<0>(o));
            st16(vact + 512, ob, pack8<1>(o));
        }
        p2_issueB<8>(P, c.w, lane, vb, sl[4]);
        WAITV(4);
        if (lane == 0) stflag(myflag, tag);
        STAMP(5);
        if (!p2on) {
            if (b == 8 * 15 && c.w == 0) {
#pragma unroll 1
                for (int i = 0; i < 4; ++i) poll((i * 64 + lane) * 128, P.ws + WS_OFLAG, tag);
                if (lane == 0) stflag(P.ws, tag);
            }
            WAITV(0);
            STAMP_OUT;
            return;
        }
    }

    const int tok0 = 256 * xcd + 64 * p2tb, p2panel = 2 * xcd + (p2tb >> 1);
#pragma unroll
    for (int i = 0; i < 4; ++i) br[3][i] = trd2(ba[i] + sl[3], ba[i] + sl[3] + 1024);
#pragma unroll
    for (int i = 0; i < 4; ++i) asm volatile("" : "+v"(br[3][i]));
    p2_issueB<7>(P, c.w, lane, vb, sl[3]);
    u32x4 xr;
    const u32 vo = (u32)(tok0 + (tid >> 3)) * 896 + p2cb * 128 + (tid & 7) * 16;
    asm volatile("global_load_dwordx4 %0, %1, %2" : "=v"(xr) : "v"(vo), "s"(P.x) : "memory");
    STAMP(6);
    const u32 vfl = (u32)(2 * (p2tb & 1) + (lane & 1) + 4 * ((lane >> 1) & 1)) * 16;
    poll(vfl, P.ws + WS_AFLAG + (p2panel * 8 + c.w) * 128, tag);
    STAMP(7);
    asm volatile("" : "+v"(xr));
    const __amdgpu_buffer_rsrc_t ra = __builtin_amdgcn_make_buffer_rsrc((void*)(P.ws + WS_ACT + (size_t)((tok0 >> 4) * 72 + 9 * c.w) * 1024), 0, 4 * 72 * 1024, 0x00020000);
    u32x4 af[6][4];
#define P2LOAD(BUF, S) _Pragma("unroll") for (int t = 0; t < 4; ++t) af[BUF][t] = __builtin_amdgcn_raw_buffer_load_b128(ra, lane * 16, (t * 72 + (S)) * 1024, 16);
    P2LOAD(0, 0) P2LOAD(1, 1) P2LOAD(2, 2) P2LOAD(3, 3) P2LOAD(4, 4) P2LOAD(5, 5)
    f32x4 acc[4][4] = {};
#define P2REG(BUF, S) _Pragma("unroll") for (int t = 0; t < 4; ++t) _Pragma("unroll") for (int i = 0; i < 4; ++i) acc[t][i] = mfma16(br[S][i], asbf(af[BUF][t]), acc[t][i]);
#define P2LDS(BUF, SL) { bf16x8 bb[4]; _Pragma("unroll") for (int i = 0; i < 4; ++i) bb[i] = trd2(ba[i] + sl[SL], ba[i] + sl[SL] + 1024); \
    _Pragma("unroll") for (int t = 0; t < 4; ++t) _Pragma("unroll") for (int i = 0; i < 4; ++i) acc[t][i] = mfma16(bb[i], asbf(af[BUF][t]), acc[t][i]); }
    P2REG(0, 0) P2LOAD(0, 6) P2REG(1, 1) P2REG(2, 2) P2REG(3, 3)
    poll(vfl, P.ws + WS_OFLAG + (p2panel * 8 + c.w) * 128, tag);
    STAMP(8);
    P2LOAD(1, 7) P2LOAD(2, 8)
    P2LDS(4, 0) P2LDS(5, 1) P2LDS(0, 2) P2LDS(1, 3) P2LDS(2, 4)
    STAMP(9);
    bar();
    {
        u32 base = c.w * 16384 + (lane & 15) * 256;
#pragma unroll
        for (int t = 0; t < 4; ++t)
#pragma unroll
            for (int i = 0; i < 4; ++i) *(LAS f32x4*)(base + t * 4096 + (((4 * i + (lane >> 4)) ^ (lane & 7)) << 4)) = acc[t][i];
    }
    bar();
    {
        int t = tid >> 3, cc = tid & 7;
        u32 a0 = t * 256 + (((2 * cc) ^ (t & 7)) << 4), a1 = t * 256 + (((2 * cc + 1) ^ (t & 7)) << 4);
        f32x4 s0 = {}, s1 = {};
#pragma unroll
        for (int w = 0; w < 8; ++w) { s0 += *(LAS f32x4*)(a0 + w * 16384); s1 += *(LAS f32x4*)(a1 + w * 16384); }
        constexpr float IL = 1.f / LOG2E;
        u32x4 r = {cvtpk(__builtin_fmaf(s0[0], IL, lo16(xr[0])), __builtin_fmaf(s0[1], IL, hi16(xr[0]))),
                   cvtpk(__builtin_fmaf(s0[2], IL, lo16(xr[1])), __builtin_fmaf(s0[3], IL, hi16(xr[1]))),
                   cvtpk(__builtin_fmaf(s1[0], IL, lo16(xr[2])), __builtin_fmaf(s1[1], IL, hi16(xr[2]))),
                   cvtpk(__builtin_fmaf(s1[2], IL, lo16(xr[3])), __builtin_fmaf(s1[3], IL, hi16(xr[3])))};
        *(GAS u32x4*)(P.out + vo) = r;
    }
    STAMP(10);
    STAMP_OUT;
}

extern "C" void kernel_launch(void* const* d_in, const int* in_sizes, int n_in, void* d_out, int out_size,
                              void* d_ws, size_t ws_size, hipStream_t stream) {
    Params P;
    P.x = (const char*)d_in[0]; P.wqkv = (const char*)d_in[1]; P.wphi = (const char*)d_in[2]; P.wo = (const char*)d_in[3];
    P.w1 = (const char*)d_in[5]; P.b1 = (const char*)d_in[6]; P.w2 = (const char*)d_in[7];
    P.out = (char*)d_out; P.ws = (char*)d_ws;
    (void)hipFuncSetAttribute((const void*)fused, hipFuncAttributeMaxDynamicSharedMemorySize, LDS_BYTES);
    fused<<<256, 512, LDS_BYTES, stream>>>(P);
}
```

```cpp
#include <cstdio>
#pragma clang diagnostic ignored "-Wint-to-pointer-cast"
#include <hip/hip_runtime.h>
#include <stdint.h>

typedef unsigned short u16;
typedef unsigned int u32;
typedef unsigned long long u64;
typedef __attribute__((ext_vector_type(8))) __bf16 bf16x8;
typedef __attribute__((ext_vector_type(2))) __bf16 bf16x2;
typedef __attribute__((ext_vector_type(4))) short s16x4;
typedef __attribute__((ext_vector_type(16))) float f32x16;
typedef __attribute__((ext_vector_type(4))) float f32x4;
typedef __attribute__((ext_vector_type(2))) float f32x2;
typedef __attribute__((ext_vector_type(16))) u32 u32x16;
typedef __attribute__((ext_vector_type(4))) u32 u32x4;
typedef __attribute__((ext_vector_type(2))) u32 u32x2;
#define LAS __attribute__((address_space(3)))
#define GAS __attribute__((address_space(1)))
#define DI __device__ __forceinline__

constexpr u32 WS_STAT = 4096, WS_OFLAG = 40960, WS_AFLAG = WS_OFLAG + 16384, WS_KVFLAG = 73728, WS_KV2FLAG = 90112;
constexpr u32 WS_KVS = 131072, WS_KVP = 262144, WS_ACT = 2u << 20;
constexpr u32 WS_DBG = 16u << 20;
constexpr u32 L_W2 = 98304, L_W3 = 32768, L_PHI = 49152, L_V = 81920, L_SM = 155648, L_CS = 159744;
constexpr u32 LDS_BYTES = 163840;
constexpr float LOG2E = 1.4426950408889634f;

struct Params { const char *x, *wqkv, *wphi, *wo, *w1, *b1, *w2; char* ws; char* out; };

#define STAMP(i)
#define STAMP_DECL
#define STAMP_OUT

DI u32x4 mkrs(const char* p) { u64 a = (u64)p; return u32x4{(u32)a, (u32)(a >> 32) & 0xffffu, 0xffffffffu, 0x00020000u}; }
DI void setm0(u32 a, u32 imm) { asm volatile("s_add_u32 m0, %0, %1\n\ts_nop 0" :: "s"(a), "i"(imm) : "memory", "scc"); }
template <int IMM> DI void bdma(u32 voff, u32x4 rs, u32 soff) {
    asm volatile("buffer_load_dwordx4 %0, %1, %2 offen offset:%3 lds" :: "v"(voff), "s"(rs), "s"(soff), "i"(IMM) : "memory");
}
#define WAITV(n) asm volatile("s_waitcnt vmcnt(" #n ")" ::: "memory")
template <int N> DI void waitv() { asm volatile("s_waitcnt vmcnt(%0)" :: "i"(N) : "memory"); }
DI void waitl() { asm volatile("s_waitcnt lgkmcnt(0)" ::: "memory"); }
DI void bar() { asm volatile("s_waitcnt lgkmcnt(0)\n\ts_barrier" ::: "memory"); }
DI void rawbar() { asm volatile("s_barrier" ::: "memory"); }
template <int IMM> DI void st16(u32 voff, char* gbase, u32x4 v) {
    asm volatile("global_store_dwordx4 %0, %1, %2 offset:%3 sc1\n\ts_nop 1" :: "v"(voff), "v"(v), "s"(gbase), "i"(IMM) : "memory");
}
template <int IMM> DI u32x4 ld16(u32 voff, const char* gbase) {
    u32x4 v;
    asm volatile("global_load_dwordx4 %0, %1, %2 offset:%3 sc1" : "=v"(v) : "v"(voff), "s"(gbase), "i"(IMM) : "memory");
    return v;
}
DI void stflag(char* gaddr, u32 tag) {
    u32 z = 0;
    asm volatile("s_mov_b64 exec, 1\n\tglobal_store_dword %0, %1, %2 sc1\n\ts_mov_b64 exec, -1" :: "v"(z), "v"(tag), "s"(gaddr) : "memory");
}
DI void st16one(u32 voff, char* gbase, u32x4 v) {
    asm volatile("s_mov_b64 exec, 1\n\tglobal_store_dwordx4 %0, %1, %2 sc1\n\ts_mov_b64 exec, -1\n\ts_nop 0" :: "v"(voff), "v"(v), "s"(gbase) : "memory");
}
DI void st16lo(u32 voff, char* gbase, u32x4 v) {
    asm volatile("s_mov_b32 exec_hi, 0\n\tglobal_store_dwordx4 %0, %1, %2 sc1\n\ts_mov_b32 exec_hi, -1\n\ts_nop 0" :: "v"(voff), "v"(v), "s"(gbase) : "memory");
}
DI void poll(u32 voff, const char* gbase, u32 tag) {
#pragma unroll 1
    for (u32 n = 0; n < (1u << 18); ++n) {
        u32 v;
        asm volatile("global_load_dword %0, %1, %2 sc1\n\ts_waitcnt vmcnt(0)" : "=v"(v) : "v"(voff), "s"(gbase) : "memory");
        if (__builtin_amdgcn_ballot_w64(v != tag) == 0) break;
        __builtin_amdgcn_s_sleep(1);
    }
}
DI u32 cvtpk(float lo, float hi) { f32x2 v = {lo, hi}; return __builtin_bit_cast(u32, __builtin_convertvector(v, bf16x2)); }
DI u32 cvtpk(f32x2 v) { return __builtin_bit_cast(u32, __builtin_convertvector(v, bf16x2)); }
DI s16x4 trd(u32 a) { return __builtin_amdgcn_ds_read_tr16_b64_v4i16((LAS s16x4*)a); }
DI bf16x8 trd2(u32 a0, u32 a1) {
    s16x4 lo = trd(a0), hi = trd(a1);
    return __builtin_bit_cast(bf16x8, __builtin_shufflevector(lo, hi, 0, 1, 2, 3, 4, 5, 6, 7));
}
DI f32x16 mfma32(bf16x8 a, bf16x8 b, f32x16 c) { return __builtin_amdgcn_mfma_f32_32x32x16_bf16(a, b, c, 0, 0, 0); }
DI f32x4 mfma16(bf16x8 a, bf16x8 b, f32x4 c) { return __builtin_amdgcn_mfma_f32_16x16x32_bf16(a, b, c, 0, 0, 0); }
DI bf16x8 asbf(u32x4 v) { return __builtin_bit_cast(bf16x8, v); }
DI float lo16(u32 v) { return __uint_as_float(v << 16); }
DI float hi16(u32 v) { return __uint_as_float(v & 0xffff0000u); }
DI f32x2 pfma(f32x2 a, f32x2 b, f32x2 c) { return __builtin_elementwise_fma(a, b, c); }
DI f32x2 splat(float v) { return f32x2{v, v}; }
template <int R> DI f32x2 pair(const f32x16& v) { return f32x2{v[R], v[R + 1]}; }
DI void halves(float v, float& a, float& b) {
    auto r = __builtin_amdgcn_permlane32_swap(__float_as_uint(v), __float_as_uint(v), false, false);
    a = __uint_as_float(r[0]); b = __uint_as_float(r[1]);
}
template <int C> DI float dppv(float v) { return __uint_as_float(__builtin_amdgcn_update_dpp(0, __float_as_uint(v), C, 0xf, 0xf, true)); }
DI float wsum(float v) {
    v += dppv<0xB1>(v); v += dppv<0x4E>(v); v += dppv<0x141>(v); v += dppv<0x140>(v);
    auto r = __builtin_amdgcn_permlane16_swap(__float_as_uint(v), __float_as_uint(v), false, false);
    v = __uint_as_float(r[0]) + __uint_as_float(r[1]);
    float a, b;
    halves(v, a, b);
    return a + b;
}
template <int I> struct IC { static constexpr int v = I; };
template <int B, int E, class F> DI void sfor(F&& f) { if constexpr (B < E) { f(IC<B>{}); sfor<B + 1, E>(f); } }
#define SB() __builtin_amdgcn_sched_barrier(0)
#define OPQ(x) asm("" : "+v"(x))
#define PIN(x) asm volatile("" : "+v"(x))
template <int S> DI u32x4 pack8(const f32x16& v) {
    u32x4 r;
    r[0] = cvtpk(v[8 * S + 0], v[8 * S + 1]); r[1] = cvtpk(v[8 * S + 2], v[8 * S + 3]);
    r[2] = cvtpk(v[8 * S + 4], v[8 * S + 5]); r[3] = cvtpk(v[8 * S + 6], v[8 * S + 7]);
    return r;
}

struct Ctx {
    int lane, w, tg, hf, h, t32;
    u32 wa[4];
    bf16x8 xf[28];
    bf16x8 mask;
    bf16x8 corrB;
    float rstd;
    u32 vb[4], m0a, m0b, sb2, sbo;
    u32x4 rb2, rbo;
};
template <int G> DI bf16x8 wread(const Ctx& c) { return trd2(c.wa[G & 3] + (G >> 2) * 1024, c.wa[G & 3] + (G >> 2) * 1024 + 512); }
template <bool FULL, class F> DI void round_mma(Ctx& c, f32x16& acc, f32x4& cs, F&& fill) {
    bf16x8 wf[4];
    wf[0] = wread<0>(c); wf[1] = wread<1>(c); wf[2] = wread<2>(c);
    SB();
    sfor<0, 28>([&](auto g_) {
        constexpr int g = g_.v;
        if constexpr (g + 3 < 28) wf[(g + 3) & 3] = wread<g + 3>(c);
        acc = mfma32(wf[g & 3], c.xf[g], g == 0 ? f32x16{} : acc);
        if constexpr (FULL || (g & 3) == 0) cs = mfma16(c.mask, wf[g & 3], g == 0 ? f32x4{} : cs);
        PIN(acc); PIN(cs);
        fill(g_);
        SB();
    });
}
template <int T> DI void cs_write(const Ctx& c, const f32x4& cs) {
    *(LAS float*)(L_CS + T * 1024 + c.hf * 512 + c.t32 * 16 + c.tg * 4) = cs[0];
}
template <int T> DI f32x4 cs_read(const Ctx& c) { return *(LAS f32x4*)(L_CS + T * 1024 + c.hf * 512 + c.t32 * 16); }
DI float sum4(f32x4 p) { float a = p[0] + p[1], b = p[2] + p[3]; OPQ(a); OPQ(b); return a + b; }
DI bf16x8 corr_a(const Ctx& c, float s, u32 bias) {
    u32 hi = cvtpk(s, s) & 0xffffu;
    float r = s - lo16(hi);
    u32x4 a;
    a[0] = cvtpk(lo16(hi), r); a[1] = hi | (bias << 16); a[2] = bias; a[3] = 0;
    if (c.h) { a[0] = 0; a[1] = 0; a[2] = 0; }
    return asbf(a);
}
template <int S, int Q> DI void issueB(const Ctx& c) {
    if constexpr (Q == 0) setm0(S % 3 == 0 ? c.m0a : c.m0b, S % 3 == 2 ? 32768 : 0);
    if constexpr (S < 7) bdma<Q * 1024>(c.vb[Q], c.rb2, c.sb2 + S * 28672);
    else {
        u32 v = c.vb[Q];
        if (S == 8 && Q == 3) v -= c.h * (c.w == 7 ? 4 * 896 : 0);
        bdma<Q * 1024>(v, c.rbo, c.sbo + (S - 7) * 25088);
    }
}
template <int S> DI void issueB4(const Ctx& c) { issueB<S, 0>(c); issueB<S, 1>(c); issueB<S, 2>(c); issueB<S, 3>(c); }
struct BAddr { u32 a[4], b[4]; };
template <int SL> DI void bread(const BAddr& ba, bf16x8 (&d)[4]) {
#pragma unroll
    for (int i = 0; i < 4; ++i) { u32 a = (SL == 0 ? ba.a[i] : ba.b[i]) + (SL == 2 ? 32768 : 0); d[i] = trd2(a, a + 1024); }
}
DI void bpin(bf16x8 (&d)[4]) { PIN(d[0]); PIN(d[1]); PIN(d[2]); PIN(d[3]); }

constexpr int mat(int g) { for (int e = 0; e < 16; ++e) if (5 + 4 * e / 3 == g) return e; return -1; }
struct Mish {
    f32x4 p; u32 bias; bf16x8 ca; float rl, z[3], x[2], o[2]; u32x4 pk;
    template <int G, int T> DI void step(Ctx& c, f32x16& e, u32 biasa, char* dst, u32 voff) {
        if constexpr (G == 0) { p = cs_read<T>(c); bias = *(LAS u16*)biasa; }
        if constexpr (G == 2) { ca = corr_a(c, sum4(p), bias); rl = c.rstd * LOG2E; }
        if constexpr (G == 3) e = mfma32(ca, c.corrB, e);
        constexpr int A = mat(G), B = mat(G - 1), C = mat(G - 2);
        if constexpr (C >= 0) {
            o[C & 1] = __builtin_fmaf(-z[C % 3], x[C & 1], z[C % 3]); OPQ(o[C & 1]);
            if constexpr (C & 1) pk[(C >> 1) & 3] = cvtpk(o[0], o[1]);
            if constexpr ((C & 7) == 7) st16<(C >> 3) * 512>(voff, dst, pk);
        }
        if constexpr (B >= 0) {
            float t = __builtin_fmaf(x[B & 1], 0.70710678118f, 0.70710678118f);
            x[B & 1] = __builtin_amdgcn_rcpf(__builtin_fmaf(t, t, 0.5f));
        }
        if constexpr (A >= 0) { z[A % 3] = e[A] * rl; OPQ(z[A % 3]); x[A & 1] = __builtin_amdgcn_exp2f(z[A % 3]); }
    }
};
template <int S, bool PK, class F> DI void mish8(const f32x16& e, float rl, char* dst, u32 voff, F&& hook) {
    f32x2 z[4], x[4];
    sfor<0, 4>([&](auto i_) {
        constexpr int i = i_.v;
        if constexpr (PK) z[i] = pair<8 * S + 2 * i>(e) * splat(rl);
        else { float a = e[8 * S + 2 * i] * rl, b = e[8 * S + 2 * i + 1] * rl; OPQ(a); OPQ(b); z[i] = f32x2{a, b}; }
        x[i] = f32x2{__builtin_amdgcn_exp2f(z[i][0]), __builtin_amdgcn_exp2f(z[i][1])};
    });
    SB(); hook(IC<0>{}); SB();
#pragma unroll
    for (int i = 0; i < 4; ++i) {
        f32x2 u;
        if constexpr (PK) { f32x2 t = pfma(x[i], splat(0.70710678118f), splat(0.70710678118f)); u = pfma(t, t, splat(0.5f)); }
        else {
            float a = __builtin_fmaf(x[i][0], 0.70710678118f, 0.70710678118f), b = __builtin_fmaf(x[i][1], 0.70710678118f, 0.70710678118f);
            OPQ(a); OPQ(b);
            a = __builtin_fmaf(a, a, 0.5f); b = __builtin_fmaf(b, b, 0.5f);
            OPQ(a); OPQ(b);
            u = f32x2{a, b};
        }
        x[i] = f32x2{__builtin_amdgcn_rcpf(u[0]), __builtin_amdgcn_rcpf(u[1])};
    }
    SB(); hook(IC<1>{}); SB();
    u32x4 pk;
#pragma unroll
    for (int i = 0; i < 4; ++i) {
        if constexpr (PK) pk[i] = cvtpk(pfma(-z[i], x[i], z[i]));
        else {
            float a = __builtin_fmaf(-z[i][0], x[i][0], z[i][0]), b = __builtin_fmaf(-z[i][1], x[i][1], z[i][1]);
            OPQ(a); OPQ(b);
            pk[i] = cvtpk(a, b);
        }
    }
    st16<S * 512>(voff, dst, pk);
    SB(); hook(IC<2>{}); SB();
}
template <class F> DI void hedge(const Ctx& c, const f32x16& e, bf16x8 wA0, bf16x8 wA1, f32x2 (&ep)[8], f32x2 (&en)[8], F&& hook) {
    f32x16 t = mfma32(wA0, asbf(pack8<0>(e)), f32x16{});
    t = mfma32(wA1, asbf(pack8<1>(e)), t);
    SB(); hook(IC<0>{}); SB();
    float mx = __builtin_fmaxf(t[0], t[1]), mn = __builtin_fminf(t[0], t[1]);
#pragma unroll
    for (int r = 2; r < 16; r += 2) {
        mx = __builtin_fmaxf(__builtin_fmaxf(mx, t[r]), t[r + 1]);
        mn = __builtin_fminf(__builtin_fminf(mn, t[r]), t[r + 1]);
    }
    const float sc = c.rstd * LOG2E;
    float a, b;
    halves(mx, a, b); float bp = -__builtin_fmaxf(a, b) * sc;
    halves(mn, a, b); float bn = __builtin_fminf(a, b) * sc;
    float bq = c.h ? -__builtin_inff() : bp, bm = c.h ? -__builtin_inff() : bn;
    SB(); hook(IC<1>{}); SB();
    sfor<0, 4>([&](auto q_) {
        constexpr int q = q_.v;
#pragma unroll
        for (int i = 2 * q; i < 2 * q + 2; ++i) {
            f32x2 v = {t[2 * i], t[2 * i + 1]};
            f32x2 p = pfma(v, splat(sc), splat(i < 6 ? bp : bq)), n = pfma(v, splat(-sc), splat(i < 6 ? bn : bm));
            ep[i] = f32x2{__builtin_amdgcn_exp2f(p[0]), __builtin_amdgcn_exp2f(p[1])};
            en[i] = f32x2{__builtin_amdgcn_exp2f(n[0]), __builtin_amdgcn_exp2f(n[1])};
        }
        SB(); hook(IC<2 + q>{}); SB();
    });
}

DI const char* mkp(u32 lo, u32 hi) { u64 p = ((u64)hi << 32) | lo; asm("" : "+s"(p)); return (const char*)p; }
template <int C> DI void issueW(u32 m0w, u32 vw0, u32x4 rw, u32 sw, u32 s64) {
    if constexpr ((C & 3) == 0) setm0(m0w, C * 1024);
    bdma<(C & 3) * 1024>(vw0, rw, sw + C * s64 - (C & 3) * 1024);
}
constexpr int rawait(int i) { return i < 2 ? 11 : i == 2 ? 10 : i == 6 ? 9 : 8; }

__global__ void __launch_bounds__(512, 2) fused(Params) {
    u32x16 ka; u32x2 kb;
    asm volatile("s_load_dwordx16 %0, %2, 0x0\n\ts_load_dwordx2 %1, %2, 0x40" : "=&s"(ka), "=&s"(kb) : "s"(__builtin_amdgcn_kernarg_segment_ptr()));
    Ctx c;
    int tid = threadIdx.x;
    c.lane = tid & 63; c.w = __builtin_amdgcn_readfirstlane(tid >> 6);
    c.tg = c.w & 3; c.hf = c.w >> 2; c.h = c.lane >> 5; c.t32 = c.lane & 31;
    const int b = blockIdx.x, xcd = b & 7, slot = b >> 3;
    const int panel = 8 * (xcd & 1) + (slot & 7), cq = xcd >> 1, kind = slot >> 3;
    const bool att = kind < 2;
    const int grp = 2 * cq + (kind & 1), odd = grp & 1;
    int lane = c.lane, g16 = lane >> 4, q4 = (lane & 15) >> 2, p4 = lane & 3;
    STAMP_DECL STAMP(0);

    const u32 stride = att ? 2688u : 3584u, s64 = 64 * stride;
    u32 vw0, vw1;
    {
        int sc = (lane & 7) ^ ((g16 & 1) << 2);
        if (att && grp == 7 && sc == 7) sc = 6;
        vw0 = (u32)(8 * c.w + (lane >> 3)) * stride + sc * 16 + 2048;
        vw1 = vw0 + (att ? 896 : 128) - 1024;
    }
    const u32 vx0 = (u32)(16 * c.w + (lane >> 3)) * 896 + (((lane & 7) ^ g16) << 4);
    const u32 vx1 = (u32)(16 * c.w + 8 + (lane >> 3)) * 896 + (((lane & 7) ^ (4 + g16)) << 4) - 1024;
    const int stok = 8 * ((att ? 0 : 8) + grp) + c.w;
    const u32 vrow = (u32)stok * 896 + (lane < 55 ? lane : 55) * 16;
    const u32 w4k = c.w * 4096;
    asm volatile("s_waitcnt lgkmcnt(0)" : "+s"(ka), "+s"(kb));
    const char *px = mkp(ka[0], ka[1]), *pqkv = mkp(ka[2], ka[3]), *pphi = mkp(ka[4], ka[5]), *pwo = mkp(ka[6], ka[7]);
    const char *pw1 = mkp(ka[8], ka[9]), *pb1 = mkp(ka[10], ka[11]), *pw2 = mkp(ka[12], ka[13]);
    char *ws = (char*)mkp(ka[14], ka[15]), *pout = (char*)mkp(kb[0], kb[1]);
    u32 tag;
    asm volatile("s_load_dword %0, %1, 0x0" : "=s"(tag) : "s"(ws));
    const char* xb = px + (size_t)panel * 128 * 896;
    char* pst = ws + WS_STAT + panel * 2048;
    asm("" : "+s"(pst));
    const u32x4 rx = mkrs(xb);
    const char* wbase = (att ? pqkv + 896 + 112 * grp : pw1 + 448 * grp + odd * 64) - 4096;
    const u32x4 rw = mkrs(wbase);
    u32x4 xrow;
    asm volatile("global_load_dwordx4 %0, %1, %2" : "=v"(xrow) : "v"(vrow), "s"(xb) : "memory");
    auto chunk = [&](auto i_, auto o_) {
        constexpr int I = i_.v, O = o_.v;
        if constexpr (O == 0) { setm0(w4k, (I & 3) * 32768); bdma<0>(vx0, rx, 128 * I); }
        if constexpr (O == 1) bdma<2048>(vw0, rw, I * s64);
        if constexpr (O == 2) bdma<1024>(vx1, rx, 128 * I);
        if constexpr (O == 3) bdma<3072>(vw1, rw, I * s64);
    };
    if (c.w < (att ? 4 : 1)) {
        u32 lim = att ? 3120u : 432u, v = tid * 16;
        setm0(c.w * 1024, L_SM);
        bdma<0>(v < lim ? v : lim, mkrs(att ? pphi + grp * 3136 : pb1 + grp * 448), 0);
    }
    sfor<0, 4>([&](auto o_) { chunk(IC<0>{}, o_); });
    rawbar();
    sfor<0, 4>([&](auto o_) { chunk(IC<1>{}, o_); });
    {
        asm volatile("s_waitcnt vmcnt(8) lgkmcnt(0)" : "+s"(tag), "+v"(xrow));
        tag += 1;
                float v[8], s = 0.f, s2 = 0.f;
#pragma unroll
                for (int e = 0; e < 4; ++e) { v[2 * e] = lo16(xrow[e]); v[2 * e + 1] = hi16(xrow[e]); s += v[2 * e] + v[2 * e + 1]; }
                float m = wsum(lane < 56 ? s : 0.f) * (1.f / 448.f);
#pragma unroll
                for (int e = 0; e < 8; ++e) { float d = v[e] - m; s2 = __builtin_fmaf(d, d, s2); }
                float var = wsum(lane < 56 ? s2 : 0.f) * (1.f / 448.f) + 1e-5f;
                float rs = __builtin_amdgcn_rsqf(var), sd = var * rs;
                u32 mh = cvtpk(-m, sd);
                u32 ml = cvtpk(-m - lo16(mh), sd - hi16(mh));
                st16one(stok * 16, pst, u32x4{(mh & 0xffffu) | (ml << 16), (mh >> 16) | (ml & 0xffff0000u), __float_as_uint(rs), tag});
    }
    rawbar();
    sfor<0, 4>([&](auto o_) { chunk(IC<2>{}, o_); });
    u32 pf0, pf1;
    {
        const int sl = slot & 7;
        u32 v; const char* p;
        if (c.w < 2) { v = (64 * c.w + lane) * 896 + (3 + kind) * 128; p = xb; }
        else {
            int r = c.w - 2;
            u32 row = r < 2 ? 192 + 32 * sl + (lane & 31) : 56 * sl + (lane < 56 ? lane : 55);
            u32 col = r < 2 ? r * (att ? 896 : 128) + (att ? (lane >> 5) * 96 : 0) : (att ? (r & 1) * 96 - 896 : ((r & 1) ? (odd ? -128 : 384) : 256));
            v = row * stride + col + 4096; p = wbase;
        }
        asm volatile("global_load_dword %0, %1, %2" : "=v"(pf0) : "v"(v), "s"(p) : "memory");
        u32 l = (slot * 8 + c.w) * 64 + lane;
        bool o = l >= 12544;
        l = o ? l - 12544 : l;
        l = l < 12544 - 9408 * o ? l : 0;
        asm volatile("global_load_dword %0, %1, off" : "=v"(pf1) : "v"((o ? pwo : pw2) + l * 128) : "memory");
    }
    STAMP(23); SB();

    const int rot = (c.tg + 2 * c.hf) & 3;
    int T = 32 * c.tg + c.t32;
    u32 xa[4], vst;
    {
        int lb = (att ? 56 : 64) * c.hf + (g16 & 1) * 32 + p4 * 8;
        u32 wq = q4 * 128 + ((((lb >> 4) ^ ((q4 >> 1) << 2)) << 4) | (lb & 15));
#pragma unroll
        for (int j = 0; j < 4; ++j) {
            int jj = (j + rot) & 3;
            c.wa[j] = (u32)(2 * jj + c.h) * 4096 + 2048 + wq;
            xa[j] = (u32)(T >> 4) * 4096 + (T & 15) * 128 + (((2 * jj + c.h) ^ ((T >> 1) & 7)) << 4);
        }
        u32 one = ((g16 ^ q4) & 1) ? 0u : 0x3f803f80u;
        c.mask = asbf(u32x4{one, one, one, one});
    }
    int p2id;
    if (!att) p2id = (kind - 2) * 8 + (slot & 7);
    else if (kind != (cq == 3 ? 0 : 1)) p2id = 16 + (slot & 7);
    else p2id = 24 + (slot & 7);
    const bool p2on = p2id < 28;
    const int p2tb = p2on ? p2id / 7 : 0, p2cb = p2on ? p2id - 7 * p2tb : 0;
    {
        u32 vb = (u32)(lane >> 3) * 896 + (((lane & 7) ^ ((g16 & 3) << 1)) << 4);
#pragma unroll
        for (int i = 0; i < 4; ++i) c.vb[i] = vb + i * 6144;
        c.m0a = w4k; c.m0b = w4k + (att ? 32768 : 90112);
        c.rb2 = mkrs(pw2 + p2cb * 128); c.rbo = mkrs(pwo + p2cb * 128);
        c.sb2 = c.w * 224 * 896; c.sbo = c.w * 56 * 896;
    }
    const u32 sW2 = (att ? -896 : 256) + 2048, sW3 = (odd ? -128 : 384) + 2048;
    const u32 m0w = c.w * 7168;

    f32x16 acc0, acc1, acc2;
    f32x4 cs0, cs1;
    u32x4 rec;
    {
        bf16x8 wf[12];
        sfor<0, 8>([&](auto i_) {
            constexpr int I = i_.v, J = I - 1;
            auto rdw = [&](auto n_) {
                constexpr int n = n_.v;
                constexpr u32 o = (I & 1) * 32768 + (n & 1) * 1024;
                wf[(8 * I + n) % 12] = trd2(c.wa[n >> 1] + o, c.wa[n >> 1] + o + 512);
            };
            if constexpr (I < 7) {
                if constexpr (I == 2 || I == 4 || I == 6) {
#pragma unroll
                    for (int j = 0; j < 4; ++j) {
                        xa[j] += I == 4 ? -65536 : 65536; c.wa[j] += I == 4 ? -65536 : 65536;
                        PIN(xa[j]); PIN(c.wa[j]);
                    }
                }
                waitv<rawait(I)>();
                bar();
                STAMP(11 + I);
                if constexpr (I == 3) asm volatile("" :: "v"(pf0), "v"(pf1));
                SB();
            }
            auto rdx = [&](auto j_) { c.xf[4 * I + j_.v] = *(LAS bf16x8*)(xa[j_.v] + (I & 1) * 32768); };
            if constexpr (I == 0) {
                sfor<0, 4>(rdx);
                sfor<0, 8>(rdw);
                sfor<0, 4>([&](auto o_) { chunk(IC<3>{}, o_); });
                SB();
                SB();
            } else {
                sfor<0, 8>([&](auto n_) {
                    constexpr int n = n_.v;
                    bf16x8 a = wf[(8 * J + n) % 12];
                    if constexpr (n & 1) acc1 = mfma32(a, c.xf[4 * J + (n >> 1)], (J == 0 && n == 1) ? f32x16{} : acc1);
                    else acc0 = mfma32(a, c.xf[4 * J + (n >> 1)], (J == 0 && n == 0) ? f32x16{} : acc0);
                    if constexpr (n == 0) cs0 = mfma16(c.mask, a, J == 0 ? f32x4{} : cs0);
                    if constexpr (n == 1) cs1 = mfma16(c.mask, a, J == 0 ? f32x4{} : cs1);
                    if constexpr (n & 1) PIN(acc1); else PIN(acc0);
                    if constexpr (n == 0) PIN(cs0);
                    if constexpr (n == 1) PIN(cs1);
                    if constexpr (I < 7) {
                        if constexpr (n == 0) { rdx(IC<0>{}); rdw(IC<0>{}); }
                        if constexpr (n == 1) { rdw(IC<1>{}); rdx(IC<1>{}); }
                        if constexpr (n == 2) { rdw(IC<2>{}); rdw(IC<3>{}); }
                        if constexpr (n == 3) { rdx(IC<2>{}); rdw(IC<4>{}); }
                        if constexpr (n == 4) { rdw(IC<5>{}); rdx(IC<3>{}); }
                        if constexpr (n == 5) { rdw(IC<6>{}); rdw(IC<7>{}); }
                    }
                    if constexpr ((n & 1) == 0) {
                        constexpr int o = n >> 1;
                        if constexpr (I == 5 && o == 0) { PIN(tid); vst = (tid & 31) * 16 + c.tg * 512; }
                        if constexpr (I == 5 && o == 0) rec = ld16<0>(vst, pst);
                        if constexpr (I <= 3) chunk(IC<I + 3>{}, IC<o>{});
                        if constexpr (I == 4) issueW<o>(m0w + L_W2, vw0, rw, sW2, s64);
                        if constexpr (I == 5 && o < 3) issueW<4 + o>(m0w + L_W2, vw0, rw, sW2, s64);
                        if constexpr (I == 5 && o == 3) issueB<0, 0>(c);
                        if constexpr (I == 6 && o == 0) issueB<0, 1>(c);
                        if constexpr (I == 6 && o == 2) issueB<0, 2>(c);
                        if constexpr (I == 7 && o == 0) issueB<0, 3>(c);
                    }
                    SB();
                });
            }
        });
    }
    PIN(tid);
    lane = c.lane = tid & 63; c.h = lane >> 5; c.t32 = lane & 31;
    g16 = lane >> 4; q4 = (lane & 15) >> 2; p4 = lane & 3; T = 32 * c.tg + c.t32;
    cs_write<0>(c, cs0);
    cs_write<1>(c, cs1);
#pragma unroll
    for (int j = 0; j < 4; ++j) c.wa[j] += c.h * 3072 + (((j + rot) & 3) * 6144 + L_W2 - 65536 - 2048);
    STAMP(1);
    if (att) WAITV(7); else WAITV(3);
    PIN(rec);
#pragma unroll 1
    for (u32 n = 0; n < (1u << 18) && __builtin_amdgcn_ballot_w64(rec[3] != tag) != 0; ++n) {
        rec = ld16<0>(vst, pst);
        WAITV(0);
        PIN(rec);
    }
    c.corrB = asbf(u32x4{(rec[0] & 0xffffu) | (rec[0] << 16), (rec[0] >> 16) | (rec[1] << 16), rec[1] >> 16, 0});
    c.rstd = __uint_as_float(rec[2]);
    bar();
    STAMP(2);

    char* actb = ws + WS_ACT + (size_t)((panel * 8 + 2 * c.tg) * 72 + 9 * grp) * 1024;
    const u32 vact = (u32)(c.t32 >> 4) * 72 * 1024 + c.h * 256 + (lane & 15) * 16;
    char* myflag = ws + (att ? WS_OFLAG : WS_AFLAG) + (panel * 8 + grp) * 128 + c.w * 16;
    const int tok0 = 256 * xcd + 64 * p2tb, p2panel = 2 * xcd + (p2tb >> 1);
    const u32 vfl = (u32)(p2panel * 8 + c.w) * 128 + (2 * (p2tb & 1) + (lane & 1) + 4 * ((lane >> 1) & 1)) * 16;
    const __amdgpu_buffer_rsrc_t ra = __builtin_amdgcn_make_buffer_rsrc((void*)(ws + WS_ACT + (size_t)((tok0 >> 4) * 72 + 9 * c.w) * 1024), 0, 4 * 72 * 1024, 0x00020000);
    BAddr ba;
    auto mkba = [&]() {
        int row = 16 * c.h + 4 * (g16 & 1) + q4, m = (row >> 1) & 3;
#pragma unroll
        for (int i = 0; i < 4; ++i) { u32 a = row * 128 + ((i ^ m) << 5) + p4 * 8; ba.a[i] = a + c.m0a; ba.b[i] = a + c.m0b; }
    };
    bf16x8 br[6][4];
    u32x4 af[4][4];
#define P2LOAD(BUF, S) _Pragma("unroll") for (int t = 0; t < 4; ++t) af[BUF][t] = __builtin_amdgcn_raw_buffer_load_b128(ra, lane * 16, (t * 72 + (S)) * 1024, 16);

    auto phase2 = [&](auto a_) __attribute__((always_inline)) {
    f32x4 acc[4][4] = {};
#define P2MM(BUF, BB) _Pragma("unroll") for (int t = 0; t < 4; ++t) _Pragma("unroll") for (int i = 0; i < 4; ++i) acc[t][i] = mfma16(BB[i], asbf(af[BUF][t]), acc[t][i]); SB();
    P2MM(0, br[0]) P2LOAD(0, 4) SB(); P2MM(1, br[1]) P2LOAD(1, 5) SB();
    P2MM(2, br[2])
    u32 fl;
    asm volatile("global_load_dword %0, %1, %2 sc1" : "=v"(fl) : "v"(vfl), "s"(ws + WS_OFLAG) : "memory");
    const u32 vo = (u32)(tok0 + (tid >> 3)) * 896 + p2cb * 128 + (tid & 7) * 16;
    const u32x4 xr = *(const GAS u32x4*)(px + vo);
    P2LOAD(2, 6) SB();
    P2MM(3, br[3])
    WAITV(5);
    PIN(fl);
    if (__builtin_amdgcn_ballot_w64(fl != tag) != 0) poll(vfl, ws + WS_OFLAG, tag);
    STAMP(8);
    P2LOAD(3, 7) SB();
    P2MM(0, br[4])
    P2LOAD(0, 8)
    bread<0>(ba, br[0]); bread<1>(ba, br[1]); bread<2>(ba, br[2]);
    SB();
    P2MM(1, br[5]) P2MM(2, br[0]) P2MM(3, br[1]) P2MM(0, br[2])
    STAMP(9);
    {
        const int l15 = lane & 15;
        u32 sx = ((c.w >> 1) & 1) | ((l15 >> 3) << 1) | (((l15 >> 1) & 3) << 2);
        u32 a = c.w * 8192 + (l15 >> 1) * 256 + (((l15 ^ c.w) & 1) << 7) + ((g16 ^ sx) << 3);
        u32x2 pk[4][4];
#pragma unroll
        for (int t = 0; t < 4; ++t)
#pragma unroll
            for (int i = 0; i < 4; ++i) pk[t][i] = u32x2{cvtpk(acc[t][i][0], acc[t][i][1]), cvtpk(acc[t][i][2], acc[t][i][3])};
        SB();
        bar();
#pragma unroll
        for (int t = 0; t < 4; ++t)
#pragma unroll
            for (int i = 0; i < 4; ++i) *(LAS u32x2*)((a ^ (i << 5)) + t * 2048) = pk[t][i];
    }
    {
        u32 sx = ((q4 >> 1) & 1) | ((c.w & 1) << 1) | (p4 << 2);
        u32 a = q4 * 8192 + (4 * c.w + p4) * 256 + ((q4 & 1) << 7) + (((2 * g16) ^ sx) << 3);
        bar();
        f32x4 d[2] = {};
#pragma unroll
        for (int j = 0; j < 4; ++j) {
            u32 one = (lane & 15) == 4 * j + g16 ? 0x3f803f80u : 0u;
            bf16x8 mj = asbf(u32x4{one, one, one, one});
#pragma unroll
            for (int e = 0; e < 2; ++e) { u32 aa = a ^ (e << 3) ^ (j << 6); d[e] = mfma16(trd2(aa, aa + 32768), mj, d[e]); }
        }
        constexpr float IL = 1.f / LOG2E;
        u32x4 r = {cvtpk(__builtin_fmaf(d[0][0], IL, lo16(xr[0])), __builtin_fmaf(d[0][1], IL, hi16(xr[0]))),
                   cvtpk(__builtin_fmaf(d[0][2], IL, lo16(xr[1])), __builtin_fmaf(d[0][3], IL, hi16(xr[1]))),
                   cvtpk(__builtin_fmaf(d[1][0], IL, lo16(xr[2])), __builtin_fmaf(d[1][1], IL, hi16(xr[2]))),
                   cvtpk(__builtin_fmaf(d[1][2], IL, lo16(xr[3])), __builtin_fmaf(d[1][3], IL, hi16(xr[3])))};
        *(GAS u32x4*)(pout + vo) = r;
    }
    STAMP(10);
    STAMP_OUT;
    };

    if (!att) {
        const u32 biasa = L_SM + (32 * (c.hf + odd) + c.t32) * 2;
        char* actw = actb + (c.hf + odd) * 1024;
        {
            Mish mi;
            round_mma<false>(c, acc2, cs0, [&](auto g_) {
                constexpr int g = g_.v;
                mi.step<g, 0>(c, acc0, biasa, actw, vact);
                if constexpr ((g & 1) == 0 && g < 14) issueW<g / 2>(m0w + L_W3, vw0, rw, sW3, s64);
            });
        }
        cs_write<2>(c, cs0);
        STAMP(3);
        WAITV(2);
        bar();
        const float rl = c.rstd * LOG2E;
        if (c.hf == odd) {
#pragma unroll
            for (int j = 0; j < 4; ++j) { c.wa[j] -= 65536; PIN(c.wa[j]); }
            Mish mi;
            round_mma<true>(c, acc0, cs0, [&](auto g_) {
                constexpr int g = g_.v;
                mi.step<g, 1>(c, acc1, biasa + 128, actw + 2048, vact);
                if constexpr ((g & 1) == 0 && g < 16) issueB<1 + g / 8, (g / 2) & 3>(c);
            });
            STAMP(4);
            mkba();
            f32x4 p = cs_read<2>(c);
            u32 b2 = *(LAS u16*)(biasa + 256), b3 = *(LAS u16*)(L_SM + ((odd ? 0 : 192) + c.t32) * 2);
            WAITV(2);
            bread<0>(ba, br[0]); bread<1>(ba, br[1]); bread<2>(ba, br[2]);
            acc2 = mfma32(corr_a(c, sum4(p), b2), c.corrB, acc2);
            acc0 = mfma32(corr_a(c, cs0[0], b3), c.corrB, acc0);
            bpin(br[0]); bpin(br[1]); bpin(br[2]);
            char* actd = actb + (odd ? 0 : 6144);
            mish8<0, true>(acc2, rl, actw + 4096, vact, [&](auto) {});
            mish8<1, true>(acc2, rl, actw + 4096, vact, [&](auto) {});
            mish8<0, true>(acc0, rl, actd, vact, [&](auto) {});
            mish8<1, true>(acc0, rl, actd, vact, [&](auto) {});
            STAMP(21);
            issueB4<3>(c); issueB4<4>(c); issueB4<5>(c);
            WAITV(12);
            stflag(myflag, tag);
            STAMP(22);
            WAITV(1);
            bread<0>(ba, br[3]); bread<1>(ba, br[4]); bread<2>(ba, br[5]);
            bpin(br[3]); bpin(br[4]); bpin(br[5]);
            issueB4<6>(c); issueB4<7>(c); issueB4<8>(c);
            STAMP(5);
            poll(vfl, ws + WS_AFLAG, tag);
            STAMP(7);
            P2LOAD(0, 0) P2LOAD(1, 1) P2LOAD(2, 2) P2LOAD(3, 3)
            phase2(IC<0>{});
            __builtin_amdgcn_endpgm();
        } else {
            mkba();
            f32x4 p1 = cs_read<1>(c), p2 = cs_read<2>(c);
            u32 b1 = *(LAS u16*)(biasa + 128), b2 = *(LAS u16*)(biasa + 256);
            bread<0>(ba, br[0]);
            acc1 = mfma32(corr_a(c, sum4(p1), b1), c.corrB, acc1);
            acc2 = mfma32(corr_a(c, sum4(p2), b2), c.corrB, acc2);
            bpin(br[0]);
            mish8<0, false>(acc1, rl, actw + 2048, vact, [&](auto) {});
            mish8<1, false>(acc1, rl, actw + 2048, vact, [&](auto) {});
            mish8<0, false>(acc2, rl, actw + 4096, vact, [&](auto) {});
            mish8<1, false>(acc2, rl, actw + 4096, vact, [&](auto) {});
            STAMP(4);
            issueB4<1>(c); issueB4<2>(c); issueB4<3>(c);
            WAITV(12);
            stflag(myflag, tag);
            STAMP(20);
            WAITV(1);
            bread<1>(ba, br[1]); bread<2>(ba, br[2]); bread<0>(ba, br[3]);
            bpin(br[1]); bpin(br[2]); bpin(br[3]);
            issueB4<4>(c); issueB4<5>(c); issueB4<6>(c);
            WAITV(4);
            bread<1>(ba, br[4]); bread<2>(ba, br[5]);
            bpin(br[4]); bpin(br[5]);
            issueB4<7>(c); issueB4<8>(c);
        }
        STAMP(5);
        poll(vfl, ws + WS_AFLAG, tag);
        STAMP(7);
        P2LOAD(0, 0) P2LOAD(1, 1) P2LOAD(2, 2) P2LOAD(3, 3)
        phase2(IC<0>{});
        __builtin_amdgcn_endpgm();
    } else {
        const int head = 2 * grp + c.hf;
        bf16x8 wA0, wA1;
        {
            int pc = 4 * (g16 & 1) + p4;
            u32 a = L_SM + c.hf * 1568 + (4 * c.h + q4) * 56 + (pc < 7 ? pc : 6) * 8;
            wA0 = trd2(a, a + 448);
            u32x4 t = __builtin_bit_cast(u32x4, trd2(a + 896, a + 1344));
            if (c.h) { t[2] = 0; t[3] = 0; }
            wA1 = asbf(t);
        }
        {
            f32x4 p0 = cs_read<0>(c), p1 = cs_read<1>(c);
            acc0 = mfma32(corr_a(c, sum4(p0), 0), c.corrB, acc0);
            acc1 = mfma32(corr_a(c, sum4(p1), 0), c.corrB, acc1);
            f32x2 ep[8], en[8];
            hedge(c, acc0, wA0, wA1, ep, en, [&](auto k_) {
                if constexpr (k_.v == 0) {
                    u32 base = L_V + c.hf * 8192 + T * 64, g = (T >> 1) & 7;
#pragma unroll
                    for (int q = 0; q < 4; ++q) {
                        f32x2 u = f32x2{acc1[4 * q], acc1[4 * q + 1]} * splat(c.rstd), v = f32x2{acc1[4 * q + 2], acc1[4 * q + 3]} * splat(c.rstd);
                        if (q == 3 && c.h) u[0] = 1.f;
                        *(LAS u32x2*)(base + (((2 * q + c.h) ^ g) << 3)) = u32x2{cvtpk(u), cvtpk(v)};
                    }
                }
            });
            f32x2 sp = ((ep[0] + ep[1]) + (ep[2] + ep[3])) + ((ep[4] + ep[5]) + (ep[6] + ep[7]));
            f32x2 sn = ((en[0] + en[1]) + (en[2] + en[3])) + ((en[4] + en[5]) + (en[6] + en[7]));
            float a, b2;
            halves(sp[0] + sp[1], a, b2); float rp = __builtin_amdgcn_rcpf(a + b2);
            halves(sn[0] + sn[1], a, b2); float rn = __builtin_amdgcn_rcpf(a + b2);
            const u32 pg = (T & 1) | (((T >> 2) & 3) << 1) | (((T >> 1) & 1) << 3);
            const u32 pbase = L_PHI + c.hf * 16384 + T * 128;
#pragma unroll
            for (int q = 0; q < 4; ++q) {
                *(LAS u32x2*)(pbase + (((2 * q + c.h) ^ pg) << 3)) = u32x2{cvtpk(ep[2 * q] * splat(rp)), cvtpk(ep[2 * q + 1] * splat(rp))};
                *(LAS u32x2*)(pbase + (((8 + 2 * q + c.h) ^ pg) << 3)) = u32x2{cvtpk(en[2 * q] * splat(rn)), cvtpk(en[2 * q + 1] * splat(rn))};
            }
        }
        WAITV(3);
        bar();
        STAMP(18);
        const bool kvw = (c.tg >> 1) == c.hf;
        const int sgn = c.tg & 1;
        if (kvw) {
            u32 pa[2], va[2];
#pragma unroll
            for (int s = 0; s < 2; ++s) {
                int Tl = 8 * c.h + 4 * s + q4;
                u32 g = (Tl & 1) | (((Tl >> 2) & 3) << 1) | (((Tl >> 1) & 1) << 3);
                pa[s] = L_PHI + c.hf * 16384 + Tl * 128 + (((8 * sgn + 4 * (g16 & 1) + p4) ^ g) << 3);
                va[s] = L_V + c.hf * 8192 + Tl * 64 + (((4 * (g16 & 1) + p4) ^ ((Tl >> 1) & 7)) << 3);
            }
            bf16x8 fa[8], fb[8];
#pragma unroll
            for (int ks = 0; ks < 8; ++ks) { fa[ks] = trd2(pa[0] + ks * 2048, pa[1] + ks * 2048); fb[ks] = trd2(va[0] + ks * 1024, va[1] + ks * 1024); }
            SB();
            f32x16 kv = {};
#pragma unroll
            for (int ks = 0; ks < 8; ++ks) kv = mfma32(fa[ks], fb[ks], kv);
            bar();
            char* kp = ws + WS_KVP + (size_t)((head * 16 + panel) * 4 + 2 * sgn) * 1024;
            st16<0>(lane * 16, kp, pack8<0>(kv));
            st16<1024>(lane * 16, kp, pack8<1>(kv));
        }
        u32x4 gk[8];
        const bool red = c.w == 2;
        const int rh = 2 * grp + (panel >> 3), ro = (panel & 7) * 512;
        const char* kfl = ws + WS_KVFLAG + grp * 2048 + ((panel >> 3) * 2 + ((panel >> 2) & 1)) * 16;
        auto gather = [&]() __attribute__((always_inline)) {
            const char* kp = ws + WS_KVP + rh * 65536 + ro;
#pragma unroll
            for (int i = 0; i < 8; ++i) gk[i] = ld16<0>(c.h * 4096 + c.t32 * 16, kp + i * 8192);
        };
        u32 rfl;
        bool rok = false;
        round_mma<false>(c, acc2, cs0, [&](auto g_) {
            constexpr int g = g_.v;
            if constexpr (g == 5) { if (!kvw) rawbar(); }
            if constexpr (g == 22) { if (red) asm volatile("global_load_dword %0, %1, %2 sc1" : "=v"(rfl) : "v"((lane & 15) * 128), "s"(kfl) : "memory"); }
            if constexpr (g == 27) {
                if (red) {
                    WAITV(0);
                    PIN(rfl);
                    rok = __builtin_amdgcn_ballot_w64(rfl != tag) == 0;
                    if (rok) gather();
                }
            }
            if constexpr (g == 10) {
                if (kvw) {
                    WAITV(2);
                    stflag(ws + WS_KVFLAG + (grp * 16 + panel) * 128 + (c.hf * 2 + sgn) * 16, tag);
                }
            }
            if constexpr (g >= 6 && g < 22 && (g & 1) == 0) issueB<1 + (g - 6) / 8, ((g - 6) / 2) & 3>(c);
        });
        cs_write<2>(c, cs0);
        STAMP(3);
        bar();
        mkba();
        if (red) {
            if (!rok) { poll((lane & 15) * 128, kfl, tag); gather(); }
        } else WAITV(0);
        STAMP(4);
        bread<0>(ba, br[0]); bread<1>(ba, br[1]); bread<2>(ba, br[2]);
        acc2 = mfma32(corr_a(c, sum4(cs_read<2>(c)), 0), c.corrB, acc2);
        bpin(br[0]); bpin(br[1]); bpin(br[2]);
        bf16x8 fq[4];
        {
            f32x2 ep[8], en[8];
            hedge(c, acc2, wA0, wA1, ep, en, [&](auto k_) {
                constexpr int k = k_.v, n = 2 * k;
                if constexpr (k == 0) {
                    if (red) {
                        WAITV(0);
                        f32x2 s[4] = {};
#pragma unroll
                        for (int i = 0; i < 8; ++i) {
                            PIN(gk[i]);
#pragma unroll
                            for (int e = 0; e < 4; ++e) s[e] += f32x2{lo16(gk[i][e]), hi16(gk[i][e])};
                        }
                        u32x4 r;
#pragma unroll
                        for (int e = 0; e < 4; ++e) {
                            float a0, b0, a1, b1;
                            halves(s[e][0], a0, b0); halves(s[e][1], a1, b1);
                            r[e] = cvtpk(a0 + b0, a1 + b1);
                        }
                        st16lo(lane * 16, ws + WS_KVS + rh * 4096 + ro, r);
                    }
                }
                if constexpr (k == 4) {
                    if (red) {
                        WAITV(8);
                        stflag(ws + WS_KV2FLAG + (grp * 16 + panel) * 128, tag);
                    }
                }
                issueB<3 + n / 4, n & 3>(c); issueB<3 + n / 4, (n & 3) + 1>(c);
            });
#pragma unroll
            for (int s = 0; s < 2; ++s) {
                fq[s] = asbf(u32x4{cvtpk(ep[4 * s]), cvtpk(ep[4 * s + 1]), cvtpk(ep[4 * s + 2]), cvtpk(ep[4 * s + 3])});
                fq[2 + s] = asbf(u32x4{cvtpk(en[4 * s]), cvtpk(en[4 * s + 1]), cvtpk(en[4 * s + 2]), cvtpk(en[4 * s + 3])});
            }
        }
        f32x4 sp = mfma16(c.mask, fq[1], mfma16(c.mask, fq[0], f32x4{})), sn = mfma16(c.mask, fq[3], mfma16(c.mask, fq[2], f32x4{}));
        STAMP(19);
        WAITV(0);
        bread<0>(ba, br[3]); bread<1>(ba, br[4]); bread<2>(ba, br[5]);
        float rp = __builtin_amdgcn_rcpf(sp[0]), rn = __builtin_amdgcn_rcpf(sn[0]);
        bpin(br[3]); bpin(br[4]); bpin(br[5]);
        issueB4<6>(c); issueB4<7>(c); issueB4<8>(c);
        STAMP(20);
        bool aok;
        {
            u32 va = WS_AFLAG + vfl, vk = WS_KV2FLAG + (grp * 16 + 8 * c.hf + (lane & 7)) * 128;
            OPQ(va); OPQ(vk);
            va = (lane & 8) ? va : vk;
            u64 m = 0;
#pragma unroll 1
            for (u32 n = 0; n < (1u << 18); ++n) {
                u32 v;
                asm volatile("global_load_dword %0, %1, %2 sc1\n\ts_waitcnt vmcnt(0)" : "=v"(v) : "v"(va), "s"(ws) : "memory");
                m = __builtin_amdgcn_ballot_w64(v != tag);
                if ((m & 255) == 0) break;
                __builtin_amdgcn_s_sleep(1);
            }
            aok = m == 0;
        }
        STAMP(7);
        u32x4 ks[4];
        {
            const char* kp = ws + WS_KVS + head * 4096;
            ks[0] = ld16<0>(lane * 16, kp); ks[1] = ld16<1024>(lane * 16, kp); ks[2] = ld16<2048>(lane * 16, kp); ks[3] = ld16<3072>(lane * 16, kp);
        }
        WAITV(0);
        PIN(ks[0]); PIN(ks[1]); PIN(ks[2]); PIN(ks[3]);
        {
            f32x16 np = mfma32(asbf(ks[1]), fq[1], mfma32(asbf(ks[0]), fq[0], f32x16{}));
            f32x16 nn = mfma32(asbf(ks[3]), fq[3], mfma32(asbf(ks[2]), fq[2], f32x16{}));
            float a, d;
            halves(__builtin_fmaf(np[12], rp, nn[12] * rn), a, d);
            float rd = __builtin_amdgcn_rcpf(d + 1e-6f) * LOG2E;
            rp *= rd; rn *= rd;
            u32x4 o[2];
            sfor<0, 8>([&](auto i_) {
                constexpr int i = i_.v;
                o[i >> 2][i & 3] = cvtpk(pfma(pair<2 * i>(np), splat(rp), pair<2 * i>(nn) * splat(rn)));
            });
            if (c.h) { o[1][2] = 0; o[1][3] = 0; }
            char* ob = actb + (7 + c.hf) * 1024;
            st16<0>(vact, ob, o[0]);
            st16<512>(vact, ob, o[1]);
        }
        STAMP(5);
        if (!p2on) {
            WAITV(0);
            stflag(myflag, tag);
            if (b == 8 * 15 && c.w == 0) {
#pragma unroll 1
                for (int i = 0; i < 4; ++i) poll((i * 64 + lane) * 128, ws + WS_OFLAG, tag);
                stflag(ws, tag);
            }
            STAMP_OUT;
            __builtin_amdgcn_endpgm();
        }
        if (!aok) {
            WAITV(0);
            stflag(myflag, tag);
            poll(vfl, ws + WS_AFLAG, tag);
        }
        P2LOAD(0, 0) P2LOAD(1, 1)
        WAITV(8);
        stflag(myflag, tag);
        STAMP(6);
        P2LOAD(2, 2) P2LOAD(3, 3)
        phase2(IC<1>{});
    }

}

extern "C" void kernel_launch(void* const* d_in, const int* in_sizes, int n_in, void* d_out, int out_size,
                              void* d_ws, size_t ws_size, hipStream_t stream) {
    Params P;
    P.x = (const char*)d_in[0]; P.wqkv = (const char*)d_in[1]; P.wphi = (const char*)d_in[2]; P.wo = (const char*)d_in[3];
    P.w1 = (const char*)d_in[5]; P.b1 = (const char*)d_in[6]; P.w2 = (const char*)d_in[7];
    P.out = (char*)d_out; P.ws = (char*)d_ws;
    fprintf(stderr, "PTRS"); for (int i = 0; i < 8; ++i) fprintf(stderr, " %p", d_in[i]); fprintf(stderr, " out %p ws %p\n", d_out, d_ws);
    (void)hipFuncSetAttribute((const void*)fused, hipFuncAttributeMaxDynamicSharedMemorySize, LDS_BYTES);
    fused<<<256, 512, LDS_BYTES, stream>>>(P);
}
```

```cpp
#pragma clang diagnostic ignored "-Wint-to-pointer-cast"
#include <hip/hip_runtime.h>
#include <stdint.h>

typedef unsigned short u16;
typedef unsigned int u32;
typedef unsigned long long u64;
typedef __attribute__((ext_vector_type(8))) __bf16 bf16x8;
typedef __attribute__((ext_vector_type(2))) __bf16 bf16x2;
typedef __attribute__((ext_vector_type(4))) short s16x4;
typedef __attribute__((ext_vector_type(16))) float f32x16;
typedef __attribute__((ext_vector_type(4))) float f32x4;
typedef __attribute__((ext_vector_type(2))) float f32x2;
typedef __attribute__((ext_vector_type(16))) u32 u32x16;
typedef __attribute__((ext_vector_type(4))) u32 u32x4;
typedef __attribute__((ext_vector_type(2))) u32 u32x2;
#define LAS __attribute__((address_space(3)))
#define GAS __attribute__((address_space(1)))
#define DI __device__ __forceinline__

constexpr unsigned FS = 16, CPF = 31744, CPFS = 128;
constexpr int PSL = 1, KSL = 1, GAT = 22, ASTP = 256;
constexpr u32 FL = 8 * FS;
constexpr u32 WS_STAT = 4096, WS_AFLAG = 40960 + 128 * FL;
constexpr u32 WS_KVS = 131072, WS_KVP = 262144, WS_ACT = 2u << 20;
constexpr u32 AST = 72 * 1024 + ASTP;
constexpr u32 L_W2 = 98304, L_W3 = 32768, L_PHI = 49152, L_V = 81920, L_SM = 155648, L_CS = 159744;
constexpr u32 LDS_BYTES = 163840;
constexpr float LOG2E = 1.4426950408889634f;

struct Params { const char *x, *wqkv, *wphi, *wo, *w1, *b1, *w2; char* ws; char* out; };

DI u32x4 mkrs(const char* p) { u64 a = (u64)p; return u32x4{(u32)a, (u32)(a >> 32) & 0xffffu, 0xffffffffu, 0x00020000u}; }
DI void setm0(u32 a, u32 imm) { asm volatile("s_add_u32 m0, %0, %1\n\ts_nop 0" :: "s"(a), "i"(imm) : "memory", "scc"); }
template <int IMM> DI void bdma(u32 voff, u32x4 rs, u32 soff) {
    asm volatile("buffer_load_dwordx4 %0, %1, %2 offen offset:%3 lds" :: "v"(voff), "s"(rs), "s"(soff), "i"(IMM) : "memory");
}
#define WAITV(n) asm volatile("s_waitcnt vmcnt(" #n ")" ::: "memory")
template <int N> DI void waitv() { asm volatile("s_waitcnt vmcnt(%0)" :: "i"(N) : "memory"); }
DI void waitl() { asm volatile("s_waitcnt lgkmcnt(0)" ::: "memory"); }
DI void bar() { asm volatile("s_waitcnt lgkmcnt(0)\n\ts_barrier" ::: "memory"); }
DI void rawbar() { asm volatile("s_barrier" ::: "memory"); }
template <int IMM> DI void st16(u32 voff, char* gbase, u32x4 v) {
    asm volatile("global_store_dwordx4 %0, %1, %2 offset:%3 sc1\n\ts_nop 1" :: "v"(voff), "v"(v), "s"(gbase), "i"(IMM) : "memory");
}
template <int IMM> DI u32x4 ld16(u32 voff, const char* gbase) {
    u32x4 v;
    asm volatile("global_load_dwordx4 %0, %1, %2 offset:%3 sc1" : "=v"(v) : "v"(voff), "s"(gbase), "i"(IMM) : "memory");
    return v;
}
template <int IMM> DI void ld16r(u32x4& v, u32 voff, const char* gbase) {
    asm volatile("global_load_dwordx4 %0, %1, %2 offset:%3 sc1" : "+v"(v) : "v"(voff), "s"(gbase), "i"(IMM) : "memory");
}
DI void stflag(char* gaddr, u32 tag) {
    u32 z = 0;
    asm volatile("s_mov_b64 exec, 1\n\tglobal_store_dword %0, %1, %2 sc1\n\ts_mov_b64 exec, -1" :: "v"(z), "v"(tag), "s"(gaddr) : "memory");
}
DI void st16one(u32 voff, char* gbase, u32x4 v) {
    asm volatile("s_mov_b64 exec, 1\n\tglobal_store_dwordx4 %0, %1, %2 sc1\n\ts_mov_b64 exec, -1\n\ts_nop 0" :: "v"(voff), "v"(v), "s"(gbase) : "memory");
}
DI void st16lo(u32 voff, char* gbase, u32x4 v) {
    asm volatile("s_mov_b64 exec, 0xffff\n\tglobal_store_dwordx4 %0, %1, %2 sc1\n\ts_mov_b64 exec, -1\n\ts_nop 0" :: "v"(voff), "v"(v), "s"(gbase) : "memory");
}
DI void poll(u32 voff, const char* gbase, u32 tag, u64 mask = ~0ull) {
#pragma unroll 1
    for (u32 n = 0; n < (1u << 18); ++n) {
        u32 v;
        asm volatile("global_load_dword %0, %1, %2 sc1\n\ts_waitcnt vmcnt(0)" : "=v"(v) : "v"(voff), "s"(gbase) : "memory");
        if ((__builtin_amdgcn_ballot_w64(v != tag) & mask) == 0) break;
        __builtin_amdgcn_s_sleep(PSL);
    }
}
DI void pollhi(u32 voff, const char* gbase, u32 tag) {
#pragma unroll 1
    for (u32 n = 0; n < (1u << 18); ++n) {
        u32 v;
        asm volatile("global_load_dword %0, %1, %2 sc1\n\ts_waitcnt vmcnt(0)" : "=v"(v) : "v"(voff), "s"(gbase) : "memory");
        if (__builtin_amdgcn_ballot_w64((v ^ tag) > 0xffffu) == 0) break;
        __builtin_amdgcn_s_sleep(PSL);
    }
}
DI void ldflagr(u32& v, u32 voff, const char* gbase) {
    asm volatile("global_load_dword %0, %1, %2 sc1" : "+v"(v) : "v"(voff), "s"(gbase) : "memory");
}
DI u32 ldflag(u32 voff, const char* gbase) {
    u32 v;
    asm volatile("global_load_dword %0, %1, %2 sc1" : "=v"(v) : "v"(voff), "s"(gbase) : "memory");
    return v;
}
DI u32 cvtpk(float lo, float hi) { f32x2 v = {lo, hi}; return __builtin_bit_cast(u32, __builtin_convertvector(v, bf16x2)); }
DI u32 cvtpk(f32x2 v) { return __builtin_bit_cast(u32, __builtin_convertvector(v, bf16x2)); }
DI s16x4 trd(u32 a) { return __builtin_amdgcn_ds_read_tr16_b64_v4i16((LAS s16x4*)a); }
DI bf16x8 trd2(u32 a0, u32 a1) {
    s16x4 lo = trd(a0), hi = trd(a1);
    return __builtin_bit_cast(bf16x8, __builtin_shufflevector(lo, hi, 0, 1, 2, 3, 4, 5, 6, 7));
}
DI f32x16 mfma32(bf16x8 a, bf16x8 b, f32x16 c) { return __builtin_amdgcn_mfma_f32_32x32x16_bf16(a, b, c, 0, 0, 0); }
DI f32x4 mfma16(bf16x8 a, bf16x8 b, f32x4 c) { return __builtin_amdgcn_mfma_f32_16x16x32_bf16(a, b, c, 0, 0, 0); }
DI bf16x8 asbf(u32x4 v) { return __builtin_bit_cast(bf16x8, v); }
DI float lo16(u32 v) { return __uint_as_float(v << 16); }
DI float hi16(u32 v) { return __uint_as_float(v & 0xffff0000u); }
DI f32x2 pfma(f32x2 a, f32x2 b, f32x2 c) { return __builtin_elementwise_fma(a, b, c); }
DI f32x2 splat(float v) { return f32x2{v, v}; }
template <int R> DI f32x2 pair(const f32x16& v) { return f32x2{v[R], v[R + 1]}; }
DI void halves(float v, float& a, float& b) {
    auto r = __builtin_amdgcn_permlane32_swap(__float_as_uint(v), __float_as_uint(v), false, false);
    a = __uint_as_float(r[0]); b = __uint_as_float(r[1]);
}
template <int C> DI float dppv(float v) { return __uint_as_float(__builtin_amdgcn_update_dpp(0, __float_as_uint(v), C, 0xf, 0xf, true)); }
DI float wsum(float v) {
    v += dppv<0xB1>(v); v += dppv<0x4E>(v); v += dppv<0x141>(v); v += dppv<0x140>(v);
    auto r = __builtin_amdgcn_permlane16_swap(__float_as_uint(v), __float_as_uint(v), false, false);
    v = __uint_as_float(r[0]) + __uint_as_float(r[1]);
    float a, b;
    halves(v, a, b);
    return a + b;
}
template <int I> struct IC { static constexpr int v = I; };
template <int B, int E, class F> DI void sfor(F&& f) { if constexpr (B < E) { f(IC<B>{}); sfor<B + 1, E>(f); } }
#define SB() __builtin_amdgcn_sched_barrier(0)
#define OPQ(x) asm("" : "+v"(x))
#define PIN(x) asm volatile("" : "+v"(x))
template <int S> DI u32x4 pack8(const f32x16& v) {
    u32x4 r;
    r[0] = cvtpk(v[8 * S + 0], v[8 * S + 1]); r[1] = cvtpk(v[8 * S + 2], v[8 * S + 3]);
    r[2] = cvtpk(v[8 * S + 4], v[8 * S + 5]); r[3] = cvtpk(v[8 * S + 6], v[8 * S + 7]);
    return r;
}

struct Ctx {
    int lane, w, tg, hf, h, t32;
    u32 wa[4];
    bf16x8 xf[28];
    bf16x8 mask;
    bf16x8 corrB;
    float rstd;
    u32 vb[4], vo3, m0a, m0b, sb2, sbo;
    u32x4 rb2, rbo;
};
template <int G> DI bf16x8 wread(const Ctx& c) { return trd2(c.wa[G & 3] + (G >> 2) * 1024, c.wa[G & 3] + (G >> 2) * 1024 + 512); }
template <bool FULL, class F> DI void round_mma(Ctx& c, f32x16& acc, f32x4& cs, F&& fill) {
    bf16x8 wf[4];
    wf[0] = wread<0>(c); wf[1] = wread<1>(c); wf[2] = wread<2>(c);
    SB();
    sfor<0, 28>([&](auto g_) {
        constexpr int g = g_.v;
        if constexpr (g + 3 < 28) wf[(g + 3) & 3] = wread<g + 3>(c);
        acc = mfma32(wf[g & 3], c.xf[g], g == 0 ? f32x16{} : acc);
        if constexpr (FULL || (g & 3) == 0) cs = mfma16(c.mask, wf[g & 3], g == 0 ? f32x4{} : cs);
        PIN(acc); PIN(cs);
        fill(g_);
        SB();
    });
}
template <int T> DI void cs_write(const Ctx& c, const f32x4& cs) {
    *(LAS float*)(L_CS + T * 1024 + c.hf * 512 + c.t32 * 16 + c.tg * 4) = cs[0];
}
template <int T> DI f32x4 cs_read(const Ctx& c) { return *(LAS f32x4*)(L_CS + T * 1024 + c.hf * 512 + c.t32 * 16); }
DI float sum4(f32x4 p) { float a = p[0] + p[1], b = p[2] + p[3]; OPQ(a); OPQ(b); return a + b; }
DI bf16x8 corr_a(const Ctx& c, float s, u32 bias) {
    u32 hi = cvtpk(s, s) & 0xffffu;
    float r = s - lo16(hi);
    u32x4 a;
    a[0] = cvtpk(lo16(hi), r); a[1] = hi | (bias << 16); a[2] = bias; a[3] = 0;
    if (c.h) { a[0] = 0; a[1] = 0; a[2] = 0; }
    return asbf(a);
}
constexpr int beta(int s) { return s < 3 ? 2 * s + 1 : s < 7 ? 2 * (s - 3) : s; }
template <int S, int Q> DI void issueB(const Ctx& c) {
    if constexpr (Q == 0) setm0(S % 3 == 0 ? c.m0a : c.m0b, S % 3 == 2 ? 32768 : 0);
    if constexpr (S < 7) bdma<Q * 1024>(c.vb[Q], c.rb2, c.sb2 + beta(S) * 28672);
    else {
        u32 v = c.vb[Q];
        if (Q == 3) v = c.vo3;
        bdma<Q * 1024>(v, c.rbo, c.sbo + (S - 7) * 25088);
    }
}
template <int S> DI void issueB4(const Ctx& c) { issueB<S, 0>(c); issueB<S, 1>(c); issueB<S, 2>(c); issueB<S, 3>(c); }
struct BAddr { u32 a[4], b[4]; };
template <int SL> DI void bread(const BAddr& ba, bf16x8 (&d)[4]) {
#pragma unroll
    for (int i = 0; i < 4; ++i) { u32 a = (SL == 0 ? ba.a[i] : ba.b[i]) + (SL == 2 ? 32768 : 0); d[i] = trd2(a, a + 1024); }
}
DI void bpin(bf16x8 (&d)[4]) { PIN(d[0]); PIN(d[1]); PIN(d[2]); PIN(d[3]); }

constexpr int mat(int g) { for (int e = 0; e < 16; ++e) if (5 + 4 * e / 3 == g) return e; return -1; }
struct Mish {
    f32x4 p; u32 bias; bf16x8 ca; float rl, z[3], x[2], o[2]; u32x4 pk;
    template <int G, int T> DI void step(Ctx& c, f32x16& e, u32 biasa, char* dst, u32 voff) {
        if constexpr (G == 0) { p = cs_read<T>(c); bias = *(LAS u16*)biasa; }
        if constexpr (G == 2) { ca = corr_a(c, sum4(p), bias); rl = c.rstd * LOG2E; }
        if constexpr (G == 3) e = mfma32(ca, c.corrB, e);
        constexpr int A = mat(G), B = mat(G - 1), C = mat(G - 2);
        if constexpr (C >= 0) {
            o[C & 1] = __builtin_fmaf(-z[C % 3], x[C & 1], z[C % 3]); OPQ(o[C & 1]);
            if constexpr (C & 1) pk[(C >> 1) & 3] = cvtpk(o[0], o[1]);
            if constexpr ((C & 7) == 7) st16<(C >> 3) * 512>(voff, dst, pk);
        }
        if constexpr (B >= 0) {
            float t = __builtin_fmaf(x[B & 1], 0.70710678118f, 0.70710678118f);
            x[B & 1] = __builtin_amdgcn_rcpf(__builtin_fmaf(t, t, 0.5f));
        }
        if constexpr (A >= 0) { z[A % 3] = e[A] * rl; OPQ(z[A % 3]); x[A & 1] = __builtin_amdgcn_exp2f(z[A % 3]); }
    }
};
template <int S, bool PK, class F> DI void mish8(const f32x16& e, float rl, char* dst, u32 voff, F&& hook) {
    f32x2 z[4], x[4];
    sfor<0, 4>([&](auto i_) {
        constexpr int i = i_.v;
        if constexpr (PK) z[i] = pair<8 * S + 2 * i>(e) * splat(rl);
        else { float a = e[8 * S + 2 * i] * rl, b = e[8 * S + 2 * i + 1] * rl; OPQ(a); OPQ(b); z[i] = f32x2{a, b}; }
        x[i] = f32x2{__builtin_amdgcn_exp2f(z[i][0]), __builtin_amdgcn_exp2f(z[i][1])};
    });
    SB(); hook(IC<0>{}); SB();
#pragma unroll
    for (int i = 0; i < 4; ++i) {
        f32x2 u;
        if constexpr (PK) { f32x2 t = pfma(x[i], splat(0.70710678118f), splat(0.70710678118f)); u = pfma(t, t, splat(0.5f)); }
        else {
            float a = __builtin_fmaf(x[i][0], 0.70710678118f, 0.70710678118f), b = __builtin_fmaf(x[i][1], 0.70710678118f, 0.70710678118f);
            OPQ(a); OPQ(b);
            a = __builtin_fmaf(a, a, 0.5f); b = __builtin_fmaf(b, b, 0.5f);
            OPQ(a); OPQ(b);
            u = f32x2{a, b};
        }
        x[i] = f32x2{__builtin_amdgcn_rcpf(u[0]), __builtin_amdgcn_rcpf(u[1])};
    }
    SB(); hook(IC<1>{}); SB();
    u32x4 pk;
#pragma unroll
    for (int i = 0; i < 4; ++i) {
        if constexpr (PK) pk[i] = cvtpk(pfma(-z[i], x[i], z[i]));
        else {
            float a = __builtin_fmaf(-z[i][0], x[i][0], z[i][0]), b = __builtin_fmaf(-z[i][1], x[i][1], z[i][1]);
            OPQ(a); OPQ(b);
            pk[i] = cvtpk(a, b);
        }
    }
    st16<S * 512>(voff, dst, pk);
    SB(); hook(IC<2>{}); SB();
}
template <class F> DI void hedge(const Ctx& c, const f32x16& e, bf16x8 wA0, bf16x8 wA1, f32x2 (&ep)[8], f32x2 (&en)[8], F&& hook) {
    f32x16 t = mfma32(wA0, asbf(pack8<0>(e)), f32x16{});
    t = mfma32(wA1, asbf(pack8<1>(e)), t);
    SB(); hook(IC<0>{}); SB();
    float mx = __builtin_fmaxf(t[0], t[1]), mn = __builtin_fminf(t[0], t[1]);
#pragma unroll
    for (int r = 2; r < 16; r += 2) {
        mx = __builtin_fmaxf(__builtin_fmaxf(mx, t[r]), t[r + 1]);
        mn = __builtin_fminf(__builtin_fminf(mn, t[r]), t[r + 1]);
    }
    const float sc = c.rstd * LOG2E;
    float a, b;
    halves(mx, a, b); float bp = -__builtin_fmaxf(a, b) * sc;
    halves(mn, a, b); float bn = __builtin_fminf(a, b) * sc;
    float bq = c.h ? -__builtin_inff() : bp, bm = c.h ? -__builtin_inff() : bn;
    SB(); hook(IC<1>{}); SB();
    sfor<0, 4>([&](auto q_) {
        constexpr int q = q_.v;
#pragma unroll
        for (int i = 2 * q; i < 2 * q + 2; ++i) {
            f32x2 v = {t[2 * i], t[2 * i + 1]};
            f32x2 p = pfma(v, splat(sc), splat(i < 6 ? bp : bq)), n = pfma(v, splat(-sc), splat(i < 6 ? bn : bm));
            ep[i] = f32x2{__builtin_amdgcn_exp2f(p[0]), __builtin_amdgcn_exp2f(p[1])};
            en[i] = f32x2{__builtin_amdgcn_exp2f(n[0]), __builtin_amdgcn_exp2f(n[1])};
        }
        SB(); hook(IC<2 + q>{}); SB();
    });
}

DI const char* mkp(u32 lo, u32 hi) { u64 p = ((u64)hi << 32) | lo; asm("" : "+s"(p)); return (const char*)p; }
template <int C> DI void issueW(u32 m0w, u32 vw0, u32x4 rw, u32 sw, u32 s64) {
    if constexpr ((C & 3) == 0) setm0(m0w, C * 1024);
    bdma<(C & 3) * 1024>(vw0, rw, sw + C * s64 - (C & 3) * 1024);
}
constexpr int rawait(int i) { return i < 2 ? 10 : i == 2 || i == 6 ? 9 : 8; }

__global__ void __launch_bounds__(512, 2) fused(Params) {
    u32x16 ka; u32x2 kb;
    asm volatile("s_load_dwordx16 %0, %2, 0x0\n\ts_load_dwordx2 %1, %2, 0x40" : "=&s"(ka), "=&s"(kb) : "s"(__builtin_amdgcn_kernarg_segment_ptr()));
    u64 pc0;
    asm volatile("s_getpc_b64 %0" : "=s"(pc0));
    pc0 &= ~127ull;
    Ctx c;
    int tid = threadIdx.x;
    c.lane = tid & 63; c.w = __builtin_amdgcn_readfirstlane(tid >> 6);
    c.tg = c.w & 3; c.hf = c.w >> 2; c.h = c.lane >> 5; c.t32 = c.lane & 31;
    const int b = blockIdx.x, xcd = b & 7, slot = b >> 3;
    const int panel = 8 * (xcd & 1) + (slot & 7), cq = xcd >> 1, kind = slot >> 3;
    const bool att = kind < 2;
    const int grp = 2 * cq + (kind & 1), odd = grp & 1;
    int lane = c.lane, g16 = lane >> 4, q4 = (lane & 15) >> 2, p4 = lane & 3;

    const u32 stride = att ? 2688u : 3584u, s64 = 64 * stride;
    u32 vw0, vw1;
    {
        int sc = (lane & 7) ^ ((g16 & 1) << 2);
        if (att && grp == 7 && sc == 7) sc = 6;
        vw0 = (u32)(8 * c.w + (lane >> 3)) * stride + sc * 16 + 2048;
        vw1 = vw0 + (att ? 896 : 128) - 1024;
    }
    const u32 vx0 = (u32)(16 * c.w + (lane >> 3)) * 896 + (((lane & 7) ^ g16) << 4);
    const u32 vx1 = (u32)(16 * c.w + 8 + (lane >> 3)) * 896 + (((lane & 7) ^ (4 + g16)) << 4) - 1024;
    const int stok = 8 * ((att ? 0 : 8) + grp) + c.w;
    const u32 vrow = (u32)stok * 896 + (lane < 55 ? lane : 55) * 16;
    const u32 w4k = c.w * 4096;
    asm volatile("s_waitcnt lgkmcnt(0)" : "+s"(ka), "+s"(kb));
    const char *px = mkp(ka[0], ka[1]), *pqkv = mkp(ka[2], ka[3]), *pphi = mkp(ka[4], ka[5]), *pwo = mkp(ka[6], ka[7]);
    const char *pw1 = mkp(ka[8], ka[9]), *pb1 = mkp(ka[10], ka[11]), *pw2 = mkp(ka[12], ka[13]);
    char *ws = (char*)mkp(ka[14], ka[15]), *pout = (char*)mkp(kb[0], kb[1]);
    u32 tag;
    asm volatile("s_load_dword %0, %1, 0x0" : "=s"(tag) : "s"(ws));
    const char* xb = px + (size_t)panel * 128 * 896;
    char* pst = ws + WS_STAT + panel * 2048;
    asm("" : "+s"(pst));
    const u32x4 rx = mkrs(xb);
    const char* wbase = (att ? pqkv + 896 + 112 * grp : pw1 + 448 * grp + odd * 64) - 4096;
    const u32x4 rw = mkrs(wbase);
    u32x4 xrow;
    asm volatile("global_load_dwordx4 %0, %1, %2" : "=v"(xrow) : "v"(vrow), "s"(xb) : "memory");
    auto chunk = [&](auto i_, auto o_) {
        constexpr int I = i_.v, O = o_.v;
        if constexpr (O == 0) { setm0(w4k, (I & 3) * 32768); bdma<0>(vx0, rx, 128 * I); }
        if constexpr (O == 1) bdma<2048>(vw0, rw, I * s64);
        if constexpr (O == 2) bdma<1024>(vx1, rx, 128 * I);
        if constexpr (O == 3) bdma<3072>(vw1, rw, I * s64);
    };
    if (c.w < (att ? 4 : 1)) {
        u32 lim = att ? 3120u : 432u, v = tid * 16;
        setm0(c.w * 1024, L_SM);
        bdma<0>(v < lim ? v : lim, mkrs(att ? pphi + grp * 3136 : pb1 + grp * 448), 0);
    }
    sfor<0, 4>([&](auto o_) { chunk(IC<0>{}, o_); });
    rawbar();
    sfor<0, 4>([&](auto o_) { chunk(IC<1>{}, o_); });
    {
        asm volatile("s_waitcnt vmcnt(8) lgkmcnt(0)" : "+s"(tag), "+v"(xrow));
        tag += 1;
                float v[8], s = 0.f, s2 = 0.f;
#pragma unroll
                for (int e = 0; e < 4; ++e) { v[2 * e] = lo16(xrow[e]); v[2 * e + 1] = hi16(xrow[e]); s += v[2 * e] + v[2 * e + 1]; }
                float m = wsum(lane < 56 ? s : 0.f) * (1.f / 448.f);
#pragma unroll
                for (int e = 0; e < 8; ++e) { float d = v[e] - m; s2 = __builtin_fmaf(d, d, s2); }
                float var = wsum(lane < 56 ? s2 : 0.f) * (1.f / 448.f) + 1e-5f;
                float rs = __builtin_amdgcn_rsqf(var), sd = var * rs;
                u32 mh = cvtpk(-m, sd);
                u32 ml = cvtpk(-m - lo16(mh), sd - hi16(mh));
                st16one(stok * 16, pst, u32x4{(mh & 0xffffu) | (ml << 16), (mh >> 16) | (ml & 0xffff0000u), __float_as_uint(rs), tag});
    }
    rawbar();
    sfor<0, 4>([&](auto o_) { chunk(IC<2>{}, o_); });
    u32 pf0 = 0, pf1 = 0;
    {
        u32 v = tid * CPFS;
        v = v < CPF ? v : CPF;
        asm volatile("global_load_dword %0, %1, %2" : "=v"(pf0) : "v"(v), "s"(pc0) : "memory");
    }
 SB();

    const int rot = (c.tg + 2 * c.hf) & 3;
    int T = 32 * c.tg + c.t32;
    u32 xa[4], vst;
    {
        int lb = (att ? 56 : 64) * c.hf + (g16 & 1) * 32 + p4 * 8;
        u32 wq = q4 * 128 + ((((lb >> 4) ^ ((q4 >> 1) << 2)) << 4) | (lb & 15));
#pragma unroll
        for (int j = 0; j < 4; ++j) {
            int jj = (j + rot) & 3;
            c.wa[j] = (u32)(2 * jj + c.h) * 4096 + 2048 + wq;
            xa[j] = (u32)(T >> 4) * 4096 + (T & 15) * 128 + (((2 * jj + c.h) ^ ((T >> 1) & 7)) << 4);
        }
        u32 one = ((g16 ^ q4) & 1) ? 0u : 0x3f803f80u;
        c.mask = asbf(u32x4{one, one, one, one});
    }
    const int p2id = slot & 15;
    const int p2cb = att ? 3 + (p2id & 3) : p2id < 12 ? p2id >> 2 : p2id - 9;
    const int p2t = att ? 64 + 48 * (p2id >> 2) : p2id < 12 ? 64 * (p2id & 3) : 0;
    {
        u32 vb = (u32)(lane >> 3) * 896 + (((lane & 7) ^ ((g16 & 3) << 1)) << 4);
#pragma unroll
        for (int i = 0; i < 4; ++i) c.vb[i] = vb + i * 6144;
        c.vo3 = c.vb[3] - ((0x7F223F00u >> (4 * (lane >> 3))) & 15) * 896;
        c.m0a = w4k; c.m0b = w4k + (att ? 32768 : 90112);
        c.rb2 = mkrs(pw2 + p2cb * 128); c.rbo = mkrs(pwo + p2cb * 128);
        c.sb2 = c.w * 224 * 896; c.sbo = c.w * 56 * 896;
    }
    const u32 sW2 = (att ? -896 : 256) + 2048, sW3 = (odd ? -128 : 384) + 2048;
    const u32 m0w = c.w * 7168;

    f32x16 acc0, acc1, acc2;
    f32x4 cs0, cs1;
    u32x4 rec;
    {
        bf16x8 wf[12];
        sfor<0, 8>([&](auto i_) {
            constexpr int I = i_.v, J = I - 1;
            auto rdw = [&](auto n_) {
                constexpr int n = n_.v;
                constexpr u32 o = (I & 1) * 32768 + (n & 1) * 1024;
                wf[(8 * I + n) % 12] = trd2(c.wa[n >> 1] + o, c.wa[n >> 1] + o + 512);
            };
            if constexpr (I < 7) {
                if constexpr (I == 2 || I == 4 || I == 6) {
#pragma unroll
                    for (int j = 0; j < 4; ++j) {
                        xa[j] += I == 4 ? -65536 : 65536; c.wa[j] += I == 4 ? -65536 : 65536;
                        PIN(xa[j]); PIN(c.wa[j]);
                    }
                }
                waitv<rawait(I)>();
                bar();
                if constexpr (I == 3) asm volatile("" :: "v"(pf0), "v"(pf1));
                SB();
            }
            auto rdx = [&](auto j_) { c.xf[4 * I + j_.v] = *(LAS bf16x8*)(xa[j_.v] + (I & 1) * 32768); };
            if constexpr (I == 0) {
                sfor<0, 4>(rdx);
                sfor<0, 8>(rdw);
                sfor<0, 4>([&](auto o_) { chunk(IC<3>{}, o_); });
                SB();
                SB();
            } else {
                sfor<0, 8>([&](auto n_) {
                    constexpr int n = n_.v;
                    bf16x8 a = wf[(8 * J + n) % 12];
                    if constexpr (n & 1) acc1 = mfma32(a, c.xf[4 * J + (n >> 1)], (J == 0 && n == 1) ? f32x16{} : acc1);
                    else acc0 = mfma32(a, c.xf[4 * J + (n >> 1)], (J == 0 && n == 0) ? f32x16{} : acc0);
                    if constexpr (n == 0) cs0 = mfma16(c.mask, a, J == 0 ? f32x4{} : cs0);
                    if constexpr (n == 1) cs1 = mfma16(c.mask, a, J == 0 ? f32x4{} : cs1);
                    if constexpr (n & 1) PIN(acc1); else PIN(acc0);
                    if constexpr (n == 0) PIN(cs0);
                    if constexpr (n == 1) PIN(cs1);
                    if constexpr (I < 7) {
                        if constexpr (n == 0) { rdx(IC<0>{}); rdw(IC<0>{}); }
                        if constexpr (n == 1) { rdw(IC<1>{}); rdx(IC<1>{}); }
                        if constexpr (n == 2) { rdw(IC<2>{}); rdw(IC<3>{}); }
                        if constexpr (n == 3) { rdx(IC<2>{}); rdw(IC<4>{}); }
                        if constexpr (n == 4) { rdw(IC<5>{}); rdx(IC<3>{}); }
                        if constexpr (n == 5) { rdw(IC<6>{}); rdw(IC<7>{}); }
                    }
                    if constexpr ((n & 1) == 0) {
                        constexpr int o = n >> 1;
                        if constexpr (I == 5 && o == 0) { PIN(tid); vst = (tid & 31) * 16 + c.tg * 512; }
                        if constexpr (I == 5 && o == 0) rec = ld16<0>(vst, pst);
                        if constexpr (I <= 3) chunk(IC<I + 3>{}, IC<o>{});
                        if constexpr (I == 4) issueW<o>(m0w + L_W2, vw0, rw, sW2, s64);
                        if constexpr (I == 5 && o < 3) issueW<4 + o>(m0w + L_W2, vw0, rw, sW2, s64);
                        if constexpr (I == 5 && o == 3) issueB<0, 0>(c);
                        if constexpr (I == 6 && o == 0) issueB<0, 1>(c);
                        if constexpr (I == 6 && o == 2) issueB<0, 2>(c);
                        if constexpr (I == 7 && o == 0) issueB<0, 3>(c);
                    }
                    SB();
                });
            }
        });
    }
    PIN(tid);
    lane = c.lane = tid & 63; c.h = lane >> 5; c.t32 = lane & 31;
    g16 = lane >> 4; q4 = (lane & 15) >> 2; p4 = lane & 3; T = 32 * c.tg + c.t32;
    cs_write<0>(c, cs0);
    cs_write<1>(c, cs1);
#pragma unroll
    for (int j = 0; j < 4; ++j) c.wa[j] += c.h * 3072 + (((j + rot) & 3) * 6144 + L_W2 - 65536 - 2048);
    if (att) WAITV(7); else WAITV(3);
    PIN(rec);
#pragma unroll 1
    for (u32 n = 0; n < (1u << 18) && __builtin_amdgcn_ballot_w64(rec[3] != tag) != 0; ++n) {
        rec = ld16<0>(vst, pst);
        WAITV(0);
        PIN(rec);
    }
    c.corrB = asbf(u32x4{(rec[0] & 0xffffu) | (rec[0] << 16), (rec[0] >> 16) | (rec[1] << 16), rec[1] >> 16, 0});
    c.rstd = __uint_as_float(rec[2]);
    bar();
    u32 pf2;
    auto warm = [&]() __attribute__((always_inline)) {
        const int u = slot * 8 + c.w;
        const char* p = u < 196 ? pw2 + u * 8192 : pwo + (u < 245 ? u - 196 : 0) * 8192;
        asm("" : "+s"(p));
        asm volatile("global_load_dword %0, %1, %2" : "=v"(pf2) : "v"(lane * 128), "s"(p) : "memory");
    };
#define PF2USE asm volatile("" :: "v"(pf2));
#define PF2N 1

    char* actb = ws + WS_ACT + (size_t)((panel * 8 + 2 * c.tg) * AST + 9 * grp * 1024);
    const u32 vact = (u32)(c.t32 >> 4) * AST + c.h * 256 + (lane & 15) * 16;
    char* myflag = ws + WS_AFLAG + (panel * 8 + grp) * FL + c.w * FS;
    const int tok0 = 256 * xcd + p2t, gam = (tok0 >> 5) + (lane & 1);
    const u32 vfl = (u32)((gam >> 2) * 8 + c.w) * FL + ((gam & 3) + 4 * ((lane >> 1) & 1)) * FS;
    const __amdgpu_buffer_rsrc_t ra = __builtin_amdgcn_make_buffer_rsrc((void*)(ws + WS_ACT + (size_t)((tok0 >> 4) * AST + 9 * c.w * 1024)), 0, 4 * AST, 0x00020000);
    BAddr ba;
    auto mkba = [&]() {
        int row = 16 * c.h + 4 * (g16 & 1) + q4, m = (row >> 1) & 3;
#pragma unroll
        for (int i = 0; i < 4; ++i) { u32 a = row * 128 + ((i ^ m) << 5) + p4 * 8; ba.a[i] = a + c.m0a; ba.b[i] = a + c.m0b; }
    };
    bf16x8 br[6][4];
    u32x4 af[4][4];
#define NTA 3
#define P2LOAD(BUF, S) _Pragma("unroll") for (int t = 0; t < NT; ++t) af[BUF][t] = __builtin_amdgcn_raw_buffer_load_b128(ra, lane * 16, t * AST + beta(S) * 1024, 16);

    const u64 mE = (c.w & 1) ? 0x3333333333333333ull : 0xccccccccccccccccull;
    auto phase2 = [&](auto a_, bool late) __attribute__((always_inline)) {
    constexpr int NT = a_.v ? NTA : 4;
    u32 fr = tag;
    if (!late) ldflagr(fr, vfl, ws + WS_AFLAG);
    P2LOAD(0, 0) P2LOAD(1, 1) P2LOAD(2, 2) SB();
    if (!late) {
        waitv<3 * NT>();
        PIN(fr);
        if (__builtin_amdgcn_ballot_w64(fr != tag) != 0) poll(vfl, ws + WS_AFLAG, tag);
    }
    f32x4 acc[4][4] = {};
#define P2MM(BUF, BB) _Pragma("unroll") for (int t = 0; t < NT; ++t) _Pragma("unroll") for (int i = 0; i < 4; ++i) acc[t][i] = mfma16(BB[i], asbf(af[BUF][t]), acc[t][i]); SB();
    P2LOAD(3, 3) SB();
    P2MM(0, br[0]) P2LOAD(0, 4) SB(); P2MM(1, br[1]) P2LOAD(1, 5) SB();
    P2MM(2, br[2])
    const u32 vo = (u32)(tok0 - (c.w < 2 * NT ? 0 : 16) + (tid >> 3)) * 896 + p2cb * 128 + (tid & 7) * 16;
    const u32x4 xr = *(const GAS u32x4*)(px + vo);
    P2LOAD(2, 6) SB();
    P2MM(3, br[3])
    P2LOAD(3, 7) SB();
    P2MM(0, br[4])
    P2LOAD(0, 8)
    bread<0>(ba, br[0]); bread<1>(ba, br[1]); bread<2>(ba, br[2]);
    SB();
    P2MM(1, br[5]) P2MM(2, br[0])
#pragma unroll
    for (int t = 0; t < NT; ++t) { PIN(acc[t][0]); PIN(acc[t][1]); PIN(acc[t][2]); PIN(acc[t][3]); }
    {
        u32 m = 0;
#pragma unroll
        for (int t = 0; t < NT; ++t) m |= (af[3][t][3] ^ (tag << 16)) | (af[0][t][3] ^ (tag << 16));
        if (__builtin_amdgcn_ballot_w64(m > 0xffffu) != 0) {
            const char* po = ws + WS_ACT + (size_t)((tok0 >> 4) * AST + (9 * c.w + 7) * 1024);
#pragma unroll 1
            for (int i = 0; i < 2 * NT; ++i) pollhi(lane * 16 + 12, po + (i >> 1) * AST + (i & 1) * 1024, tag << 16);
#pragma unroll
            for (int t = 0; t < NT; ++t) { ld16r<0>(af[3][t], lane * 16, po + t * AST); ld16r<1024>(af[0][t], lane * 16, po + t * AST); }
            WAITV(0);
#pragma unroll
            for (int t = 0; t < NT; ++t) { PIN(af[3][t]); PIN(af[0][t]); }
        }
    }
#pragma unroll
    for (int t = 0; t < NT; ++t) { af[3][t][3] &= 0xffffu; af[0][t][3] &= 0xffffu; }
    SB();
    P2MM(3, br[1]) P2MM(0, br[2])
    {
        const int l15 = lane & 15;
        u32 sx = ((c.w >> 1) & 1) | ((l15 >> 3) << 1) | (((l15 >> 1) & 3) << 2);
        u32 a = c.m0b + (l15 >> 1) * 256 + (((l15 ^ c.w) & 1) << 7) + ((g16 ^ sx) << 3);
        u32x2 pk[4][4];
#pragma unroll
        for (int t = 0; t < NT; ++t)
#pragma unroll
            for (int i = 0; i < 4; ++i) pk[t][i] = u32x2{cvtpk(acc[t][i][0], acc[t][i][1]), cvtpk(acc[t][i][2], acc[t][i][3])};
        SB();
#pragma unroll
        for (int t = 0; t < NT; ++t)
#pragma unroll
            for (int i = 0; i < 4; ++i) *(LAS u32x2*)((a ^ (i << 5)) + (t >> 1) * 32768 + (t & 1) * 2048) = pk[t][i];
    }
    {
        u32 sx = ((q4 >> 1) & 1) | ((c.w & 1) << 1) | (p4 << 2);
        constexpr u32 HS = 16384;
        u32 a = (a_.v ? 32768 : 90112) + (c.w >> 2) * 32768 + q4 * 4096 + (4 * (c.w & 3) + p4) * 256 + ((q4 & 1) << 7) + (((2 * g16) ^ sx) << 3);
        bar();
        if (NT < 4 && c.w >= 2 * NT) { __builtin_amdgcn_endpgm(); }
        f32x4 d[2] = {};
#pragma unroll
        for (int j = 0; j < 4; ++j) {
            u32 one = (lane & 15) == 4 * j + g16 ? 0x3f803f80u : 0u;
            bf16x8 mj = asbf(u32x4{one, one, one, one});
#pragma unroll
            for (int e = 0; e < 2; ++e) { u32 aa = a ^ (e << 3) ^ (j << 6); d[e] = mfma16(trd2(aa, aa + HS), mj, d[e]); }
        }
        constexpr float IL = 1.f / LOG2E;
        u32x4 r = {cvtpk(__builtin_fmaf(d[0][0], IL, lo16(xr[0])), __builtin_fmaf(d[0][1], IL, hi16(xr[0]))),
                   cvtpk(__builtin_fmaf(d[0][2], IL, lo16(xr[1])), __builtin_fmaf(d[0][3], IL, hi16(xr[1]))),
                   cvtpk(__builtin_fmaf(d[1][0], IL, lo16(xr[2])), __builtin_fmaf(d[1][1], IL, hi16(xr[2]))),
                   cvtpk(__builtin_fmaf(d[1][2], IL, lo16(xr[3])), __builtin_fmaf(d[1][3], IL, hi16(xr[3])))};
        *(GAS u32x4*)(pout + vo) = r;
    }
    };

    if (!att) {
        const u32 biasa = L_SM + (32 * (c.hf + odd) + c.t32) * 2;
        char* actw = actb + (c.hf + odd) * 1024;
        {
            Mish mi;
            round_mma<false>(c, acc2, cs0, [&](auto g_) {
                constexpr int g = g_.v;
                mi.step<g, 0>(c, acc0, biasa, actw, vact);
                if constexpr ((g & 1) == 0 && g < 14) issueW<g / 2>(m0w + L_W3, vw0, rw, sW3, s64);
                if constexpr (g == 14) warm();
            });
        }
        cs_write<2>(c, cs0);
        waitv<2 + PF2N>();
        bar();
        const float rl = c.rstd * LOG2E;
        if (c.hf == odd) {
#pragma unroll
            for (int j = 0; j < 4; ++j) { c.wa[j] -= 65536; PIN(c.wa[j]); }
            Mish mi;
            round_mma<true>(c, acc0, cs0, [&](auto g_) {
                constexpr int g = g_.v;
                mi.step<g, 1>(c, acc1, biasa + 128, actw + 2048, vact);
                if constexpr ((g & 1) == 0 && g < 16) issueB<1 + g / 8, (g / 2) & 3>(c);
            });
            mkba();
            f32x4 p = cs_read<2>(c);
            u32 b2 = *(LAS u16*)(biasa + 256), b3 = *(LAS u16*)(L_SM + ((odd ? 0 : 192) + c.t32) * 2);
            WAITV(2);
            PF2USE
            bread<0>(ba, br[0]); bread<1>(ba, br[1]); bread<2>(ba, br[2]);
            acc2 = mfma32(corr_a(c, sum4(p), b2), c.corrB, acc2);
            acc0 = mfma32(corr_a(c, cs0[0], b3), c.corrB, acc0);
            bpin(br[0]); bpin(br[1]); bpin(br[2]);
            char* actd = actb + (odd ? 0 : 6144);
            mish8<0, true>(acc2, rl, actw + 4096, vact, [&](auto k_) {
            });
            mish8<1, true>(acc2, rl, actw + 4096, vact, [&](auto) {});
            mish8<0, true>(acc0, rl, actd, vact, [&](auto) {});
            mish8<1, true>(acc0, rl, actd, vact, [&](auto) {});
            issueB4<3>(c); issueB4<4>(c); issueB4<5>(c);
            WAITV(12);
            stflag(myflag, tag);
            WAITV(1);
            bread<0>(ba, br[3]); bread<1>(ba, br[4]); bread<2>(ba, br[5]);
            bpin(br[3]); bpin(br[4]); bpin(br[5]);
            u32 f2 = ldflag(vfl, ws + WS_AFLAG);
            issueB4<6>(c); issueB4<7>(c); issueB4<8>(c);
            rawbar();
            WAITV(12);
            PIN(f2);
            if ((__builtin_amdgcn_ballot_w64(f2 != tag) & mE) != 0) poll(vfl, ws + WS_AFLAG, tag, mE);
            phase2(IC<0>{}, false);
            __builtin_amdgcn_endpgm();
        } else {
            __builtin_amdgcn_s_setprio(1);
            mkba();
            f32x4 p1 = cs_read<1>(c), p2 = cs_read<2>(c);
            u32 b1 = *(LAS u16*)(biasa + 128), b2 = *(LAS u16*)(biasa + 256);
            bread<0>(ba, br[0]);
            acc1 = mfma32(corr_a(c, sum4(p1), b1), c.corrB, acc1);
            acc2 = mfma32(corr_a(c, sum4(p2), b2), c.corrB, acc2);
            bpin(br[0]);
            mish8<0, false>(acc1, rl, actw + 2048, vact, [&](auto) {});
            mish8<1, false>(acc1, rl, actw + 2048, vact, [&](auto) {});
            mish8<0, false>(acc2, rl, actw + 4096, vact, [&](auto) {});
            mish8<1, false>(acc2, rl, actw + 4096, vact, [&](auto) {});
            __builtin_amdgcn_s_setprio(0);
            issueB4<1>(c); issueB4<2>(c); issueB4<3>(c);
            WAITV(12);
            PF2USE
            stflag(myflag, tag);
            WAITV(1);
            bread<1>(ba, br[1]); bread<2>(ba, br[2]); bread<0>(ba, br[3]);
            bpin(br[1]); bpin(br[2]); bpin(br[3]);
            issueB4<4>(c); issueB4<5>(c); issueB4<6>(c);
            WAITV(4);
            bread<1>(ba, br[4]); bread<2>(ba, br[5]);
            bpin(br[4]); bpin(br[5]);
            issueB4<7>(c); issueB4<8>(c);
            if (b == 128 && c.w == 4) {
#pragma unroll 1
                for (int i = 0; i < 4; ++i) poll((i * 64 + lane) * 128 + 12, ws + WS_STAT, tag);
                stflag(ws, tag);
            }
        }
        poll(vfl, ws + WS_AFLAG, tag, mE);
        rawbar();
        phase2(IC<0>{}, false);
        __builtin_amdgcn_endpgm();
    } else {
        warm();
        const int head = 2 * grp + c.hf;
        bf16x8 wA0, wA1;
        {
            int pc = 4 * (g16 & 1) + p4;
            u32 a = L_SM + c.hf * 1568 + (4 * c.h + q4) * 56 + (pc < 7 ? pc : 6) * 8;
            wA0 = trd2(a, a + 448);
            u32x4 t = __builtin_bit_cast(u32x4, trd2(a + 896, a + 1344));
            if (c.h) { t[2] = 0; t[3] = 0; }
            wA1 = asbf(t);
        }
        {
            f32x4 p0 = cs_read<0>(c), p1 = cs_read<1>(c);
            acc0 = mfma32(corr_a(c, sum4(p0), 0), c.corrB, acc0);
            acc1 = mfma32(corr_a(c, sum4(p1), 0), c.corrB, acc1);
            f32x2 ep[8], en[8];
            hedge(c, acc0, wA0, wA1, ep, en, [&](auto k_) {
                if constexpr (k_.v == 0) {
                    u32 base = L_V + c.hf * 8192 + T * 64, g = (T >> 1) & 7;
#pragma unroll
                    for (int q = 0; q < 4; ++q) {
                        f32x2 u = f32x2{acc1[4 * q], acc1[4 * q + 1]} * splat(c.rstd), v = f32x2{acc1[4 * q + 2], acc1[4 * q + 3]} * splat(c.rstd);
                        if (q == 3 && c.h) u[0] = 1.f;
                        *(LAS u32x2*)(base + (((2 * q + c.h) ^ g) << 3)) = u32x2{cvtpk(u), cvtpk(v)};
                    }
                }
            });
            f32x2 sp = ((ep[0] + ep[1]) + (ep[2] + ep[3])) + ((ep[4] + ep[5]) + (ep[6] + ep[7]));
            f32x2 sn = ((en[0] + en[1]) + (en[2] + en[3])) + ((en[4] + en[5]) + (en[6] + en[7]));
            float a, b2;
            halves(sp[0] + sp[1], a, b2); float rp = __builtin_amdgcn_rcpf(a + b2);
            halves(sn[0] + sn[1], a, b2); float rn = __builtin_amdgcn_rcpf(a + b2);
            const u32 pg = (T & 1) | (((T >> 2) & 3) << 1) | (((T >> 1) & 1) << 3);
            const u32 pbase = L_PHI + c.hf * 16384 + T * 128;
#pragma unroll
            for (int q = 0; q < 4; ++q) {
                *(LAS u32x2*)(pbase + (((2 * q + c.h) ^ pg) << 3)) = u32x2{cvtpk(ep[2 * q] * splat(rp)), cvtpk(ep[2 * q + 1] * splat(rp))};
                *(LAS u32x2*)(pbase + (((8 + 2 * q + c.h) ^ pg) << 3)) = u32x2{cvtpk(en[2 * q] * splat(rn)), cvtpk(en[2 * q + 1] * splat(rn))};
            }
        }
        WAITV(3);
        bar();
        const bool kvw = (c.tg >> 1) == c.hf;
        const int sgn = c.tg & 1;
        if (kvw) {
            u32 pa[2], va[2];
#pragma unroll
            for (int s = 0; s < 2; ++s) {
                int Tl = 8 * c.h + 4 * s + q4;
                u32 g = (Tl & 1) | (((Tl >> 2) & 3) << 1) | (((Tl >> 1) & 1) << 3);
                pa[s] = L_PHI + c.hf * 16384 + Tl * 128 + (((8 * sgn + 4 * (g16 & 1) + p4) ^ g) << 3);
                va[s] = L_V + c.hf * 8192 + Tl * 64 + (((4 * (g16 & 1) + p4) ^ ((Tl >> 1) & 7)) << 3);
            }
            bf16x8 fa[8], fb[8];
#pragma unroll
            for (int ks = 0; ks < 8; ++ks) { fa[ks] = trd2(pa[0] + ks * 2048, pa[1] + ks * 2048); fb[ks] = trd2(va[0] + ks * 1024, va[1] + ks * 1024); }
            SB();
            f32x16 kv = {};
#pragma unroll
            for (int ks = 0; ks < 8; ++ks) kv = mfma32(fa[ks], fb[ks], kv);
            bar();
            char* kp = ws + WS_KVP + (size_t)((head * 16 + panel) * 2 + sgn) * 3072;
            st16<0>(lane * 16, kp, u32x4{cvtpk(kv[0], kv[1]), cvtpk(kv[2], kv[3]), cvtpk(kv[4], kv[5]), tag});
            st16<1024>(lane * 16, kp, u32x4{cvtpk(kv[6], kv[7]), cvtpk(kv[8], kv[9]), cvtpk(kv[10], kv[11]), tag});
            st16<2048>(lane * 16, kp, u32x4{cvtpk(kv[12], kv[13]), cvtpk(kv[14], kv[15]), 0, tag});
        }
        u32x4 gk[4];
        const bool red = c.w >= 2 && c.w < 5;
        const int rh = 2 * grp + (panel >> 3), ro = (panel & 7) * 768 + (c.w - 2) * 256;
        auto gather = [&]() __attribute__((always_inline)) {
            const char* kp = ws + WS_KVP + rh * 98304 + ro;
#pragma unroll
            for (int i = 0; i < 4; ++i) gk[i] = ld16<0>(g16 * 6144 + (lane & 15) * 16, kp + i * 24576);
        };
        auto fresh = [&]() __attribute__((always_inline)) {
            WAITV(0);
            PIN(gk[0]); PIN(gk[1]); PIN(gk[2]); PIN(gk[3]);
            return __builtin_amdgcn_ballot_w64(((gk[0][3] ^ tag) | (gk[1][3] ^ tag) | (gk[2][3] ^ tag) | (gk[3][3] ^ tag)) != 0) == 0;
        };
        auto reduce = [&]() __attribute__((always_inline)) {
            u32 one = 0x3f80u << (16 * (lane & 1)), q = (lane & 15) >> 1;
            OPQ(one); OPQ(q);
            const u32 e0 = q == 0 ? one : 0, e1 = q == 1 ? one : 0;
            f32x4 X = {}, Y = {};
#pragma unroll
            for (int i = 0; i < 4; ++i) {
                u32x4 v = gk[i];
                v[3] = 0;
                X = mfma16(asbf(u32x4{e0, e1, 0, 0}), asbf(v), X);
                Y = mfma16(asbf(u32x4{0, 0, e0, 0}), asbf(v), Y);
            }
            st16lo(lane * 16, ws + WS_KVS + rh * 6144 + ro, u32x4{cvtpk(X[0], X[1]), cvtpk(X[2], X[3]), cvtpk(Y[0], Y[1]), tag});
        };
        round_mma<false>(c, acc2, cs0, [&](auto g_) {
            constexpr int g = g_.v;
            if constexpr (g == 5) { if (!kvw) rawbar(); }
            if constexpr (g == GAT) { if (red) gather(); }
            if constexpr (g >= 6 && g < 22 && (g & 1) == 0) issueB<1 + (g - 6) / 8, ((g - 6) / 2) & 3>(c);
        });
        cs_write<2>(c, cs0);
        bool rok = true;
        if (red) {
            rok = fresh();
            if (rok) reduce();
        }
        bar();
        mkba();
        if (!red) WAITV(0);
        if (!rok) {
#pragma unroll 1
            for (u32 n = 0; n < (1u << 18); ++n) {
                gather();
                if (fresh()) break;
                __builtin_amdgcn_s_sleep(PSL);
            }
            reduce();
        }
        PF2USE
        bread<0>(ba, br[0]); bread<1>(ba, br[1]); bread<2>(ba, br[2]);
        acc2 = mfma32(corr_a(c, sum4(cs_read<2>(c)), 0), c.corrB, acc2);
        bpin(br[0]); bpin(br[1]); bpin(br[2]);
        bf16x8 fq[4];
        {
            f32x2 ep[8], en[8];
            hedge(c, acc2, wA0, wA1, ep, en, [&](auto k_) {
                constexpr int k = k_.v, n = 2 * k;
                if constexpr (k < 3) issueB4<3 + k>(c);
            });
#pragma unroll
            for (int s = 0; s < 2; ++s) {
                fq[s] = asbf(u32x4{cvtpk(ep[4 * s]), cvtpk(ep[4 * s + 1]), cvtpk(ep[4 * s + 2]), cvtpk(ep[4 * s + 3])});
                fq[2 + s] = asbf(u32x4{cvtpk(en[4 * s]), cvtpk(en[4 * s + 1]), cvtpk(en[4 * s + 2]), cvtpk(en[4 * s + 3])});
            }
        }
        f32x4 sp = mfma16(c.mask, fq[1], mfma16(c.mask, fq[0], f32x4{})), sn = mfma16(c.mask, fq[3], mfma16(c.mask, fq[2], f32x4{}));
        u32x4 ks[6];
        u32 afl;
        auto ksload = [&]() __attribute__((always_inline)) {
            const char* kp = ws + WS_KVS + head * 6144;
            ks[0] = ld16<0>(lane * 16, kp); ks[1] = ld16<1024>(lane * 16, kp); ks[2] = ld16<2048>(lane * 16, kp);
            ks[3] = ld16<0>(lane * 16, kp + 3072); ks[4] = ld16<1024>(lane * 16, kp + 3072); ks[5] = ld16<2048>(lane * 16, kp + 3072);
            asm volatile("global_load_dword %0, %1, %2 sc1" : "=v"(afl) : "v"(vfl), "s"(ws + WS_AFLAG) : "memory");
        };
        auto ksok = [&]() __attribute__((always_inline)) {
            PIN(ks[0]); PIN(ks[1]); PIN(ks[2]); PIN(ks[3]); PIN(ks[4]); PIN(ks[5]); PIN(afl);
            u32 m = ((ks[0][3] ^ tag) | (ks[1][3] ^ tag) | (ks[2][3] ^ tag)) | ((ks[3][3] ^ tag) | (ks[4][3] ^ tag) | (ks[5][3] ^ tag));
            return __builtin_amdgcn_ballot_w64(m != 0) == 0;
        };
        ksload();
        WAITV(7);
        bread<0>(ba, br[3]); bread<1>(ba, br[4]); bread<2>(ba, br[5]);
        float rp = __builtin_amdgcn_rcpf(sp[0]), rn = __builtin_amdgcn_rcpf(sn[0]);
        bpin(br[3]); bpin(br[4]); bpin(br[5]);
        WAITV(0);
#pragma unroll 1
        for (u32 n = 0; n < (1u << 18) && !ksok(); ++n) {
            __builtin_amdgcn_s_sleep(KSL);
            ksload();
            WAITV(0);
        }
        const u64 amiss = __builtin_amdgcn_ballot_w64(afl != tag);
        const bool aok = (amiss & mE) == 0;
        {
            f32x16 np = mfma32(asbf(u32x4{ks[1][1], ks[1][2], ks[2][0], ks[2][1]}), fq[1], mfma32(asbf(u32x4{ks[0][0], ks[0][1], ks[0][2], ks[1][0]}), fq[0], f32x16{}));
            f32x16 nn = mfma32(asbf(u32x4{ks[4][1], ks[4][2], ks[5][0], ks[5][1]}), fq[3], mfma32(asbf(u32x4{ks[3][0], ks[3][1], ks[3][2], ks[4][0]}), fq[2], f32x16{}));
            float a, d;
            halves(__builtin_fmaf(np[12], rp, nn[12] * rn), a, d);
            float rd = __builtin_amdgcn_rcpf(d + 1e-6f) * LOG2E;
            rp *= rd; rn *= rd;
            u32x4 o[2];
            sfor<0, 8>([&](auto i_) {
                constexpr int i = i_.v;
                o[i >> 2][i & 3] = cvtpk(pfma(pair<2 * i>(np), splat(rp), pair<2 * i>(nn) * splat(rn)));
            });
            o[1][2] = __builtin_amdgcn_permlane32_swap(o[1][2], o[1][3], false, false)[0];
            o[1][3] = (o[0][3] >> 16) | (tag << 16);
            o[0][3] = (o[0][3] & 0xffffu) | (tag << 16);
            char* ob = actb + (7 + c.hf) * 1024;
            st16<0>(vact, ob, o[0]);
            st16<512>(vact, ob, o[1]);
        }
        rawbar();
        issueB4<6>(c); issueB4<7>(c); issueB4<8>(c);
        if (!aok) {
            poll(vfl, ws + WS_AFLAG, tag, mE);
        }
        phase2(IC<1>{}, amiss == 0);
    }

}

extern "C" void kernel_launch(void* const* d_in, const int* in_sizes, int n_in, void* d_out, int out_size,
                              void* d_ws, size_t ws_size, hipStream_t stream) {
    Params P;
    P.x = (const char*)d_in[0]; P.wqkv = (const char*)d_in[1]; P.wphi = (const char*)d_in[2]; P.wo = (const char*)d_in[3];
    P.w1 = (const char*)d_in[5]; P.b1 = (const char*)d_in[6]; P.w2 = (const char*)d_in[7];
    P.out = (char*)d_out; P.ws = (char*)d_ws;
    (void)hipFuncSetAttribute((const void*)fused, hipFuncAttributeMaxDynamicSharedMemorySize, LDS_BYTES);
    fused<<<256, 512, LDS_BYTES, stream>>>(P);
}
```
